```python
import torch

def _softmax(x, axis=-1):
    return torch.softmax(x, dim=axis)

def _gelu(x):
    return torch.nn.functional.gelu(x, approximate='tanh')

def _silu(x):
    return torch.nn.functional.silu(x)

def _sigmoid(x):
    return torch.sigmoid(x)

def _layernorm(x, axis=-1, eps=1e-05):
    m = x.mean(dim=axis, keepdim=True)
    v = x.var(dim=axis, keepdim=True, unbiased=False)
    return (x - m) / torch.sqrt(v + eps)

def _scatter_add(tgt, idx, src, axis=0):
    return tgt.clone().index_add_(axis, idx, src)

def _segment_sum(src, seg, n):
    out = torch.zeros((n,) + src.shape[1:], dtype=src.dtype)
    return out.index_add_(0, seg, src)

def _pad(x, pw):
    flat = tuple((v for pair in reversed(pw) for v in pair))
    return torch.nn.functional.pad(x, flat)
kernel_name = 'sequential_preLN__ssm_conv_h3__mixer_gmlp__prompt'

def setup_inputs():

    _w = lambda a: (lambda t: t.to(torch.bfloat16) if t.is_floating_point() else t)(torch.as_tensor(a))

    def _A_setup():
        B, S, D = (1, 2048, 448)
        ks = 4
        return {'q': _w((lambda *s: torch.randn(*s, dtype=torch.bfloat16) * 0.05)(B, S, D)), 'k': _w((lambda *s: torch.randn(*s, dtype=torch.bfloat16) * 0.4)(B, S, D)), 'v': _w((lambda *s: torch.randn(*s, dtype=torch.bfloat16) * 0.4)(B, S, D)), 'shift_k': _w((lambda *s: torch.randn(*s, dtype=torch.bfloat16) * 0.4)(D, ks) * 0.5), 'ssm_k': _w((lambda *s: torch.randn(*s, dtype=torch.bfloat16) * 0.4)(D, S) * (1.0 / S ** 0.5)), 'ssm_D': _w((lambda *s: torch.randn(*s, dtype=torch.bfloat16) * 0.4)(D))}
    _A_in = _A_setup()

    def _B_setup():
        B, S, D, F = (1, 2048, 448, 1792)
        Fh = F // 2
        return {'x': _w((lambda *s: torch.randn(*s, dtype=torch.bfloat16) * 0.05)(B, S, D)), 'w_in': _w((lambda *s: torch.randn(*s, dtype=torch.bfloat16) * 0.4)(D, 2 * Fh)), 'w_sp': _w(0.01 * (lambda *s: torch.randn(*s, dtype=torch.bfloat16) * 0.4)(S, S)), 'b_sp': _w(1.0 + 0.01 * (lambda *s: torch.randn(*s, dtype=torch.bfloat16) * 0.05)(S)), 'w_out': _w((lambda *s: torch.randn(*s, dtype=torch.bfloat16) * 0.05)(Fh, D))}
    _B_in = _B_setup()
    return {'A_q': _A_in['q'], 'A_k': _A_in['k'], 'A_v': _A_in['v'], 'A_shift_k': _A_in['shift_k'], 'A_ssm_k': _A_in['ssm_k'], 'A_ssm_D': _A_in['ssm_D'], 'B_x': _B_in['x'], 'B_w_in': _B_in['w_in'], 'B_w_sp': _B_in['w_sp'], 'B_b_sp': _B_in['b_sp'], 'B_w_out': _B_in['w_out']}

def reference(A_q, A_k, A_v, A_shift_k, A_ssm_k, A_ssm_D, B_x, B_w_in, B_w_sp, B_b_sp, B_w_out):

    def _A_ref(q, k, v, shift_k, ssm_k, ssm_D):
        B, S, D = q.shape
        ks = shift_k.shape[-1]
        k_pad = _pad(k, ((0, 0), (ks - 1, 0), (0, 0)))
        k_shift = torch.zeros_like(k)
        for t in range(ks):
            k_shift = k_shift + k_pad[:, t:t + S, :] * shift_k[:, ks - 1 - t][None, None, :]
        kv = k_shift * v
        L = 2 * S
        kv_pad = _pad(kv, ((0, 0), (0, S), (0, 0)))
        kf_pad = _pad(ssm_k, ((0, 0), (0, S)))
        kv_f = (lambda x, axes=(-2, -1): torch.fft.rfft2(x.float(), dim=axes))(kv_pad[:, None, :, :], axes=(1, 2))
        kf_f = (lambda x, axes=(-2, -1): torch.fft.rfft2(x.float(), dim=axes))(kf_pad[:, None, :], axes=(1, 2))
        kf_f = (lambda x, *d: x.permute(*d))(kf_f, 1, 2, 0)
        y_f = kv_f * kf_f[None, :, :, :]
        y = (lambda x, s, axes=(-2, -1): torch.fft.irfft2(x, s=s, dim=axes).to(torch.bfloat16))(y_f, (1, L), axes=(1, 2))[:, 0, :S, :]
        y = y + kv * ssm_D[None, None, :]
        return q * y

    def _B_ref(x, w_in, w_sp, b_sp, w_out):
        B, S, D = x.shape
        F = w_in.shape[-1]
        Fh = F // 2
        z = _gelu(torch.einsum('bsd,df->bsf', x, w_in))
        z1 = z[..., :Fh].clone()
        z2 = _layernorm(z[..., Fh:], axis=-1)
        g = torch.einsum('ts,bsf->btf', w_sp, z2) + b_sp[None, :, None]
        return torch.einsum('bsf,fd->bsd', z1 * g, w_out)
    x = A_q
    h = x + _A_ref(_layernorm(x, axis=-1), A_k, A_v, A_shift_k, A_ssm_k, A_ssm_D)
    h2 = h + _B_ref(_layernorm(h, axis=-1), B_w_in, B_w_sp, B_b_sp, B_w_out)
    return h2
```

```cpp
#include <hip/hip_runtime.h>
#include <hip/hip_bf16.h>
#include <stdint.h>

typedef __attribute__((ext_vector_type(8))) short bf16x8;
typedef __attribute__((ext_vector_type(4))) short s16x4;
typedef __attribute__((ext_vector_type(4))) float f32x4;
typedef __attribute__((ext_vector_type(2))) float f32x2;
typedef __attribute__((ext_vector_type(4))) unsigned int u32x4;
typedef __attribute__((ext_vector_type(2))) unsigned int u32x2;
typedef __attribute__((ext_vector_type(2))) __bf16 bf16x2_t;
typedef unsigned short u16;

#define DEVI __device__ __forceinline__
#define STAMP(kid, i)

constexpr int S_ = 2048, D_ = 448, F_ = 1792, FH_ = 896;

DEVI float lo2f(unsigned v) { return __uint_as_float(v << 16); }
DEVI float hi2f(unsigned v) { return __uint_as_float(v & 0xffff0000u); }
DEVI float bf2f(u16 v) { return __uint_as_float(((unsigned)v) << 16); }
DEVI unsigned pack2(float a, float b) {
  f32x2 f = {a, b};
  bf16x2_t h = __builtin_convertvector(f, bf16x2_t);
  return __builtin_bit_cast(unsigned, h);
}
DEVI float rbf(float x) { return lo2f(pack2(x, x)); }
DEVI f32x4 mfma16(bf16x8 a, bf16x8 b, f32x4 c) {
  return __builtin_amdgcn_mfma_f32_16x16x32_bf16(a, b, c, 0, 0, 0);
}
template <int CTRL> DEVI float dpp_f(float x) {
  return __builtin_bit_cast(float, __builtin_amdgcn_update_dpp(0, __builtin_bit_cast(int, x), CTRL, 0xf, 0xf, false));
}
DEVI float row16_sum(float x) {
  x += dpp_f<0x121>(x);
  x += dpp_f<0x122>(x);
  x += dpp_f<0x124>(x);
  x += dpp_f<0x128>(x);
  return x;
}
DEVI float gelu_tanh(float x) {
  const float u = x * (0.7978845608028654f + 0.035677408136300125f * x * x);
  const float e = __builtin_amdgcn_exp2f(-2.885390081777927f * u);
  return x * __builtin_amdgcn_rcpf(1.0f + e);
}

constexpr size_t WS_YT = 0;
constexpr size_t WS_H = WS_YT + (size_t)D_ * S_ * 2;
constexpr size_t WS_WINT = WS_H + (size_t)S_ * D_ * 2;
constexpr size_t WS_WOUTT = WS_WINT + (size_t)F_ * D_ * 2;
constexpr size_t WS_LN1 = WS_WOUTT + (size_t)D_ * FH_ * 2;
constexpr size_t WS_Z1 = WS_LN1 + (size_t)S_ * 8;
constexpr size_t WS_Z2T = WS_Z1 + (size_t)S_ * FH_ * 2;
constexpr size_t WS_PST = WS_Z2T + (size_t)S_ * FH_ * 2;
constexpr size_t WS_U = WS_PST + (size_t)4 * S_ * 8;

constexpr int K1_XREV = 0;
constexpr int K1_XSTRIDE = 2304 * 2;
constexpr int K1_COP = 9216;
constexpr int K1_CSTRIDE = 4128;
constexpr int K1_RED = K1_COP + 2 * 8 * K1_CSTRIDE;
constexpr int K1_LDS = K1_RED + 8 * 8192;

__global__ __launch_bounds__(512) void k1_conv(
    const u16* __restrict__ kin, const u16* __restrict__ vin, const u16* __restrict__ shk,
    const u16* __restrict__ ssmk, const u16* __restrict__ ssmD, u16* __restrict__ yT,
    const u16* __restrict__ xin, const u16* __restrict__ w_in, const u16* __restrict__ w_out,
    u16* __restrict__ w_inT, u16* __restrict__ w_outT, float2* __restrict__ ln1) {
  __shared__ __attribute__((aligned(16))) char smem[K1_LDS];
  const int tid = threadIdx.x, lane = tid & 63, wave = tid >> 6;
  STAMP(0, 0);
  const int xcd = blockIdx.x & 7, idx = blockIdx.x >> 3;

  if (idx >= 28) {
    const int a = xcd * 4 + (idx - 28);
    {
      u32x4 xr[8];
#pragma unroll
      for (int rr = 0; rr < 8; ++rr) {
        xr[rr] = (u32x4){0u, 0u, 0u, 0u};
        if (lane < 56) xr[rr] = *(const u32x4*)(xin + (size_t)(a * 64 + wave * 8 + rr) * D_ + lane * 8);
      }
#pragma unroll
      for (int rr = 0; rr < 8; ++rr) {
        float v[8];
#pragma unroll
        for (int j = 0; j < 4; ++j) { v[2 * j] = lo2f(xr[rr][j]); v[2 * j + 1] = hi2f(xr[rr][j]); }
        float sm = 0.f;
#pragma unroll
        for (int j = 0; j < 8; ++j) sm += v[j];
#pragma unroll
        for (int o = 1; o < 64; o <<= 1) sm += __shfl_xor(sm, o);
        const float mean = sm * (1.0f / D_);
        float m2 = 0.f;
        if (lane < 56) {
#pragma unroll
          for (int j = 0; j < 8; ++j) { float d = v[j] - mean; m2 += d * d; }
        }
#pragma unroll
        for (int o = 1; o < 64; o <<= 1) m2 += __shfl_xor(m2, o);
        if (lane == 0) {
          const float var = rbf(m2 * (1.0f / D_));
          const float sd = rbf(sqrtf(rbf(var + 1e-5f)));
          ln1[a * 64 + wave * 8 + rr] = make_float2(rbf(mean), 1.0f / sd);
        }
      }
    }
    {
      typedef __attribute__((address_space(3))) s16x4* lp;
      char* tl = smem + wave * 4608;
      const int aw = a * 8 + wave;
      const int r = lane & 15, q = lane >> 4;
      u32x4 sl[3][4];
#pragma unroll
      for (int j = 0; j < 3; ++j) {
        const int id = aw + 256 * j;
        if (id < 588) {
          const u16* src; int ld;
          if (id < 392) { src = w_in + (size_t)(id / 28) * 32 * F_ + (id % 28) * 64; ld = F_; }
          else { const int i2 = id - 392; src = w_out + (size_t)(i2 / 7) * 32 * D_ + (i2 % 7) * 64; ld = D_; }
#pragma unroll
          for (int i = 0; i < 4; ++i)
            sl[j][i] = *(const u32x4*)(src + (size_t)(8 * i + (lane >> 3)) * ld + (lane & 7) * 8);
        }
      }
#pragma unroll
      for (int j = 0; j < 3; ++j) {
        const int id = aw + 256 * j;
        if (id < 588) {
          u16* dst;
          int bstride;
          if (id < 392) { dst = w_inT + (size_t)((id % 28) * 4 * 14 + (id / 28)) * 512; bstride = 14 * 512; }
          else { const int i2 = id - 392; dst = w_outT + (size_t)((i2 % 7) * 4 * 28 + (i2 / 7)) * 512; bstride = 28 * 512; }
#pragma unroll
          for (int i = 0; i < 4; ++i)
            *(u32x4*)(tl + (8 * i + (lane >> 3)) * 144 + (lane & 7) * 16) = sl[j][i];
#pragma unroll
          for (int n = 0; n < 4; ++n) {
            const char* a0p = tl + (8 * q + (r >> 2)) * 144 + (16 * n + 4 * (r & 3)) * 2;
            s16x4 v0 = __builtin_amdgcn_ds_read_tr16_b64_v4i16((lp)(a0p));
            s16x4 v1 = __builtin_amdgcn_ds_read_tr16_b64_v4i16((lp)(a0p + 4 * 144));
            bf16x8 o = {v0[0], v0[1], v0[2], v0[3], v1[0], v1[1], v1[2], v1[3]};
            *(bf16x8*)(dst + (size_t)n * bstride + lane * 8) = o;
          }
        }
      }
    }
    return;
  }

  const int d0 = (xcd * 28 + idx) * 2;
  const int s0 = tid * 4;
  unsigned kk[4], vv[4];
#pragma unroll
  for (int j = 0; j < 4; ++j) kk[j] = *(const unsigned*)(kin + (size_t)(s0 + j) * D_ + d0);
#pragma unroll
  for (int j = 0; j < 4; ++j) vv[j] = *(const unsigned*)(vin + (size_t)(s0 + j) * D_ + d0);
  const int tch = tid >> 8, mm = tid & 255;
  const u16* krow = ssmk + (size_t)(d0 + tch) * S_;
  u32x4 wa = *(const u32x4*)(krow + mm * 8);
  u32x4 wb = {0u, 0u, 0u, 0u};
  if (mm < 255) wb = *(const u32x4*)(krow + mm * 8 + 8);
  unsigned hk[3] = {0u, 0u, 0u};
  if (lane == 0 && s0 > 0) {
#pragma unroll
    for (int j = 0; j < 3; ++j) hk[j] = *(const unsigned*)(kin + (size_t)(s0 - 3 + j) * D_ + d0);
  }
  const u32x4 skw = *(const u32x4*)(shk + d0 * 4);
  const unsigned dDw = *(const unsigned*)(ssmD + d0);

  if (tid < 64) {
    u32x4 z = {0u, 0u, 0u, 0u};
    *(u32x4*)(smem + K1_XREV + (tid >> 5) * K1_XSTRIDE + (2048 + (tid & 31) * 8) * 2) = z;
  }
  {
    unsigned w[8] = {wa[0], wa[1], wa[2], wa[3], wb[0], wb[1], wb[2], wb[3]};
    unsigned al[7];
#pragma unroll
    for (int i = 0; i < 7; ++i) al[i] = (w[i] >> 16) | (w[i + 1] << 16);
    char* base = smem + K1_COP + tch * 8 * K1_CSTRIDE + (mm + 2) * 16;
#pragma unroll
    for (int c = 0; c < 8; ++c) {
      u32x4 o;
      if ((c & 1) == 0) { o[0] = w[c / 2]; o[1] = w[c / 2 + 1]; o[2] = w[c / 2 + 2]; o[3] = w[c / 2 + 3]; }
      else { o[0] = al[c / 2]; o[1] = al[c / 2 + 1]; o[2] = al[c / 2 + 2]; o[3] = al[c / 2 + 3]; }
      *(u32x4*)(base + c * K1_CSTRIDE) = o;
    }
    if (mm < 2) {
      unsigned w2[8] = {0u, 0u, 0u, 0u, 0u, 0u, 0u, 0u};
      if (mm == 0) { w2[4] = wa[0]; w2[5] = wa[1]; w2[6] = wa[2]; w2[7] = wa[3]; }
      unsigned a2[7];
#pragma unroll
      for (int i = 0; i < 7; ++i) a2[i] = (w2[i] >> 16) | (w2[i + 1] << 16);
      char* b2 = smem + K1_COP + tch * 8 * K1_CSTRIDE + (1 - mm) * 16;
#pragma unroll
      for (int c = 0; c < 8; ++c) {
        u32x4 o;
        if ((c & 1) == 0) { o[0] = w2[c / 2]; o[1] = w2[c / 2 + 1]; o[2] = w2[c / 2 + 2]; o[3] = w2[c / 2 + 3]; }
        else { o[0] = a2[c / 2]; o[1] = a2[c / 2 + 1]; o[2] = a2[c / 2 + 2]; o[3] = a2[c / 2 + 3]; }
        *(u32x4*)(b2 + c * K1_CSTRIDE) = o;
      }
    }
  }
  {
    unsigned p1 = __shfl_up(kk[3], 1), p2 = __shfl_up(kk[2], 1), p3 = __shfl_up(kk[1], 1);
    if (lane == 0) { p3 = hk[0]; p2 = hk[1]; p1 = hk[2]; }
    unsigned kw[7] = {p3, p2, p1, kk[0], kk[1], kk[2], kk[3]};
#pragma unroll
    for (int ch = 0; ch < 2; ++ch) {
      const float sk0 = lo2f(skw[2 * ch]), sk1 = hi2f(skw[2 * ch]);
      const float sk2 = lo2f(skw[2 * ch + 1]), sk3 = hi2f(skw[2 * ch + 1]);
      float kf[7];
#pragma unroll
      for (int j = 0; j < 7; ++j) kf[j] = ch ? hi2f(kw[j]) : lo2f(kw[j]);
      float kvv[4];
#pragma unroll
      for (int j = 0; j < 4; ++j) {
        float a = rbf(kf[j] * sk3);
        a = rbf(a + rbf(kf[j + 1] * sk2));
        a = rbf(a + rbf(kf[j + 2] * sk1));
        a = rbf(a + rbf(kf[j + 3] * sk0));
        const float vf = ch ? hi2f(vv[j]) : lo2f(vv[j]);
        kvv[j] = a * vf;
      }
      u32x2 o = {pack2(kvv[3], kvv[2]), pack2(kvv[1], kvv[0])};
      *(u32x2*)(smem + K1_XREV + ch * K1_XSTRIDE + (2044 - s0) * 2) = o;
    }
  }
  STAMP(0, 1);
  __syncthreads();
  STAMP(0, 2);

  {
    const int ch = wave & 1, r = lane & 15, q = lane >> 4;
    f32x4 acc[8];
#pragma unroll
    for (int t = 0; t < 8; ++t) acc[t] = (f32x4){0.f, 0.f, 0.f, 0.f};
    const char* abase = smem + K1_COP + ch * 8 * K1_CSTRIDE + ((r + 1) & 7) * K1_CSTRIDE +
                        (2 * (q >> 1) - (q & 1) + ((r + 9) >> 3)) * 16;
    const char* bbase = smem + K1_XREV + ch * K1_XSTRIDE + (2040 - 16 * r + 16 * (q >> 1) - 8 * (q & 1)) * 2;
#pragma unroll
    for (int cc = 0; cc < 2; ++cc) {
      const int rho = 2 * (wave >> 1) + cc;
      bf16x8 A[8], B[8];
#pragma unroll
      for (int i = 0; i < 8; ++i) A[i] = *(const bf16x8*)(abase + (rho + 8 * i) * 64);
#pragma unroll
      for (int i = 0; i < 8; ++i) B[i] = *(const bf16x8*)(bbase - (8 * i - rho) * 64);
#pragma unroll
      for (int t = 0; t < 8; ++t)
#pragma unroll
        for (int i = 0; i <= t; ++i) acc[t] = mfma16(A[i], B[t - i], acc[t]);
    }
    float* red = (float*)(smem + K1_RED + wave * 8192);
#pragma unroll
    for (int t = 0; t < 8; ++t) *(f32x4*)(red + 256 * t + 16 * r + 4 * q) = acc[t];
  }
  STAMP(0, 3);
  __syncthreads();
  STAMP(0, 4);
  {
    const int ch = tid >> 8, e0 = (tid & 255) * 8;
    f32x4 sa = {0.f, 0.f, 0.f, 0.f}, sb = {0.f, 0.f, 0.f, 0.f};
#pragma unroll
    for (int w = 0; w < 4; ++w) {
      const float* red = (const float*)(smem + K1_RED + (2 * w + ch) * 8192);
      sa += *(const f32x4*)(red + e0);
      sb += *(const f32x4*)(red + e0 + 4);
    }
    const u32x4 kr = *(const u32x4*)(smem + K1_XREV + ch * K1_XSTRIDE + (2040 - e0) * 2);
    const float Dv = ch ? hi2f(dDw) : lo2f(dDw);
    float kvf[8];
    kvf[7] = lo2f(kr[0]); kvf[6] = hi2f(kr[0]); kvf[5] = lo2f(kr[1]); kvf[4] = hi2f(kr[1]);
    kvf[3] = lo2f(kr[2]); kvf[2] = hi2f(kr[2]); kvf[1] = lo2f(kr[3]); kvf[0] = hi2f(kr[3]);
    float y[8];
#pragma unroll
    for (int j = 0; j < 4; ++j) {
      y[j] = rbf(sa[j]) + rbf(kvf[j] * Dv);
      y[j + 4] = rbf(sb[j]) + rbf(kvf[j + 4] * Dv);
    }
    u32x4 o = {pack2(y[0], y[1]), pack2(y[2], y[3]), pack2(y[4], y[5]), pack2(y[6], y[7])};
    *(u32x4*)(yT + (size_t)(d0 + ch) * S_ + e0) = o;
  }
  STAMP(0, 5);
}

constexpr int K2_YT = 0;
constexpr int K2_AT = 448 * 144;
constexpr int K2_ST = K2_AT + 64 * 1024;
constexpr int K2_LDS = K2_ST + 8 * 64 * 8;

__global__ __launch_bounds__(512) void k2_gemm1(
    const u16* __restrict__ xin, const u16* __restrict__ yT, const float2* __restrict__ ln1,
    const u16* __restrict__ w_inT, u16* __restrict__ hout, u16* __restrict__ z1,
    u16* __restrict__ z2T, float2* __restrict__ pstat) {
  __shared__ __attribute__((aligned(16))) char smem[K2_LDS];
  const int tid = threadIdx.x, lane = tid & 63, wave = tid >> 6;
  STAMP(1, 0);
  const int xcd = blockIdx.x & 7, idx = blockIdx.x >> 3;
  const int rb = xcd * 4 + (idx & 3), cb = idx >> 2;
  const int R0 = rb * 64, C0 = cb * 256;
  const int r = lane & 15, q = lane >> 4;

  u32x4 yl[7];
#pragma unroll
  for (int j = 0; j < 7; ++j) {
    const int id = tid + 512 * j;
    yl[j] = *(const u32x4*)(yT + (size_t)(id >> 3) * S_ + R0 + (id & 7) * 8);
  }
  const int prow = 16 * (wave & 3) + r;
  const int dc0 = (wave >> 2) * 28 + q;
  u32x4 xg[7];
#pragma unroll
  for (int j = 0; j < 7; ++j) xg[j] = *(const u32x4*)(xin + (size_t)R0 * D_ + (tid + 512 * j) * 8);
  const float2 l1 = ln1[R0 + prow];
  bf16x8 wf[2][14];
#pragma unroll
  for (int i = 0; i < 2; ++i)
#pragma unroll
    for (int ks = 0; ks < 14; ++ks)
      wf[i][ks] = *(const bf16x8*)(w_inT + (size_t)(((cb * 16 + 2 * wave + i) * 14 + ks) * 64 + lane) * 8);

#pragma unroll
  for (int j = 0; j < 7; ++j) {
    const int id = tid + 512 * j;
    *(u32x4*)(smem + K2_YT + (id >> 3) * 144 + (id & 7) * 16) = yl[j];
  }
#pragma unroll
  for (int j = 0; j < 7; ++j) {
    const int id = tid + 512 * j;
    const int row = id / 56, c = id % 56;
    *(u32x4*)(smem + K2_AT + row * 1024 + ((c ^ (row & 15)) * 16)) = xg[j];
  }
  STAMP(1, 1);
  __syncthreads();
  STAMP(1, 2);

  unsigned hp[7][4];
  float sum = 0.f;
  {
    typedef __attribute__((address_space(3))) s16x4* lp;
    const int qq = r >> 2, pp = r & 3;
#pragma unroll
    for (int it = 0; it < 7; ++it) {
      const int d = (dc0 + 4 * it) * 8;
      const char* a0 = smem + K2_YT + (d + qq) * 144 + (16 * (wave & 3) + 4 * pp) * 2;
      s16x4 y0 = __builtin_amdgcn_ds_read_tr16_b64_v4i16((lp)(a0));
      s16x4 y1 = __builtin_amdgcn_ds_read_tr16_b64_v4i16((lp)(a0 + 4 * 144));
      float yf[8];
#pragma unroll
      for (int j = 0; j < 4; ++j) { yf[j] = bf2f((u16)y0[j]); yf[j + 4] = bf2f((u16)y1[j]); }
      const u32x4 xv = *(const u32x4*)(smem + K2_AT + prow * 1024 + (((dc0 + 4 * it) ^ r) * 16));
#pragma unroll
      for (int j = 0; j < 4; ++j) {
        const float x0 = lo2f(xv[j]), x1 = hi2f(xv[j]);
        const unsigned dd = pack2(x0 - l1.x, x1 - l1.x);
        const unsigned qn = pack2(lo2f(dd) * l1.y, hi2f(dd) * l1.y);
        const unsigned aa = pack2(lo2f(qn) * yf[2 * j], hi2f(qn) * yf[2 * j + 1]);
        const unsigned hh = pack2(x0 + lo2f(aa), x1 + hi2f(aa));
        hp[it][j] = hh;
        sum += lo2f(hh) + hi2f(hh);
      }
    }
  }
  sum += __shfl_xor(sum, 16);
  sum += __shfl_xor(sum, 32);
  const float mh = sum * (1.0f / 224.0f);
  float m2 = 0.f;
#pragma unroll
  for (int it = 0; it < 7; ++it)
#pragma unroll
    for (int j = 0; j < 4; ++j) {
      const float d0 = lo2f(hp[it][j]) - mh, d1 = hi2f(hp[it][j]) - mh;
      m2 += d0 * d0 + d1 * d1;
    }
  m2 += __shfl_xor(m2, 16);
  m2 += __shfl_xor(m2, 32);
  float2* st = (float2*)(smem + K2_ST);
  if (q == 0) st[wave * 16 + r] = make_float2(mh, m2);
  STAMP(1, 3);
  __syncthreads();
  STAMP(1, 4);
  {
    const float2 o = st[(wave ^ 4) * 16 + r];
    const float mean = 0.5f * (mh + o.x);
    const float dl = mh - o.x;
    const float var = rbf((m2 + o.y + dl * dl * 112.0f) * (1.0f / D_));
    const float mb = rbf(mean);
    const float rinv = 1.0f / rbf(sqrtf(rbf(var + 1e-5f)));
#pragma unroll
    for (int it = 0; it < 7; ++it) {
      u32x4 o4;
#pragma unroll
      for (int j = 0; j < 4; ++j) {
        const unsigned dd = pack2(lo2f(hp[it][j]) - mb, hi2f(hp[it][j]) - mb);
        o4[j] = pack2(lo2f(dd) * rinv, hi2f(dd) * rinv);
      }
      const int dc = dc0 + 4 * it;
      *(u32x4*)(smem + K2_AT + prow * 1024 + ((dc ^ r) * 16)) = o4;
      if (cb == 0) {
        u32x4 h4 = {hp[it][0], hp[it][1], hp[it][2], hp[it][3]};
        *(u32x4*)(hout + (size_t)(R0 + prow) * D_ + dc * 8) = h4;
      }
    }
  }
  STAMP(1, 5);
  __syncthreads();
  STAMP(1, 6);

  const bool isZ2 = (C0 + 32 * wave) >= FH_;
  f32x4 acc[2][4];
#pragma unroll
  for (int i = 0; i < 2; ++i)
#pragma unroll
    for (int s = 0; s < 4; ++s) acc[i][s] = (f32x4){0.f, 0.f, 0.f, 0.f};
  if (isZ2) {
#pragma unroll
    for (int ks = 0; ks < 14; ++ks) {
      bf16x8 xf[4];
#pragma unroll
      for (int s = 0; s < 4; ++s)
        xf[s] = *(const bf16x8*)(smem + K2_AT + (16 * s + r) * 1024 + (((4 * ks + q) ^ r) * 16));
#pragma unroll
      for (int i = 0; i < 2; ++i)
#pragma unroll
        for (int s = 0; s < 4; ++s) acc[i][s] = mfma16(xf[s], wf[i][ks], acc[i][s]);
      if (ks & 1) __builtin_amdgcn_sched_barrier(0);
    }
    float zs[4][4], zq[4][4];
#pragma unroll
    for (int s = 0; s < 4; ++s) {
      float zz[2][4];
#pragma unroll
      for (int i = 0; i < 2; ++i) {
#pragma unroll
        for (int j = 0; j < 4; ++j) zz[i][j] = rbf(gelu_tanh(rbf(acc[i][s][j])));
        u32x2 o = {pack2(zz[i][0], zz[i][1]), pack2(zz[i][2], zz[i][3])};
        const int ftile = cb * 16 + 2 * wave + i - 56;
        const int ksg = 2 * rb + (s >> 1);
        const int lq = 2 * (s & 1) + (q >> 1);
        *(u32x2*)(z2T + (size_t)((ftile * 64 + ksg) * 64 + 16 * lq + r) * 8 + 4 * (q & 1)) = o;
      }
#pragma unroll
      for (int j = 0; j < 4; ++j) {
        const float mw = row16_sum(zz[0][j] + zz[1][j]) * (1.0f / 32.0f);
        const float a0 = zz[0][j] - mw, a1 = zz[1][j] - mw;
        zs[s][j] = mw; zq[s][j] = row16_sum(a0 * a0 + a1 * a1);
      }
    }
    if (r == 0) {
#pragma unroll
      for (int s = 0; s < 4; ++s)
#pragma unroll
        for (int j = 0; j < 4; ++j) st[wave * 64 + 16 * s + 4 * q + j] = make_float2(zs[s][j], zq[s][j]);
    }
  } else {
#pragma unroll
    for (int ks = 0; ks < 14; ++ks) {
      bf16x8 xf[4];
#pragma unroll
      for (int s = 0; s < 4; ++s)
        xf[s] = *(const bf16x8*)(smem + K2_AT + (16 * s + r) * 1024 + (((4 * ks + q) ^ r) * 16));
#pragma unroll
      for (int i = 0; i < 2; ++i)
#pragma unroll
        for (int s = 0; s < 4; ++s) acc[i][s] = mfma16(wf[i][ks], xf[s], acc[i][s]);
      if (ks & 1) __builtin_amdgcn_sched_barrier(0);
    }
#pragma unroll
    for (int s = 0; s < 4; ++s)
#pragma unroll
      for (int i = 0; i < 2; ++i) {
        float zz[4];
#pragma unroll
        for (int j = 0; j < 4; ++j) zz[j] = gelu_tanh(rbf(acc[i][s][j]));
        u32x2 o = {pack2(zz[0], zz[1]), pack2(zz[2], zz[3])};
        *(u32x2*)(z1 + (size_t)(((rb * 4 + s) * 56 + cb * 16 + 2 * wave + i) * 64 + lane) * 4) = o;
      }
  }
  if (cb >= 3) {
    __syncthreads();
    if (tid < 64) {
      const int w0 = (cb == 3) ? 4 : 0;
      float msum = 0.f;
      for (int w = w0; w < 8; ++w) msum += st[w * 64 + tid].x;
      const float mean = msum / (float)(8 - w0);
      float M2 = 0.f;
      for (int w = w0; w < 8; ++w) {
        const float2 p = st[w * 64 + tid];
        const float dl = p.x - mean;
        M2 += p.y + 32.0f * dl * dl;
      }
      pstat[(size_t)(cb - 3) * S_ + R0 + tid] = make_float2(mean, M2);
    }
  }
  STAMP(1, 7);
}

constexpr int K3_RT = 0;
constexpr int K3_MT = 8192;
constexpr int K3_WB = 12288;
constexpr int K3_CB = K3_WB + 3 * 16384;
constexpr int K3_LDS = K3_CB + 256;
constexpr int K3_D = 4;
constexpr int K3_DZ = 3;

__global__ __launch_bounds__(512) void k3_gemm2(
    const u16* __restrict__ w_sp, const u16* __restrict__ b_sp, const u16* __restrict__ z2T,
    const u16* __restrict__ z1, const float2* __restrict__ pstat, u16* __restrict__ uout) {
  __shared__ __attribute__((aligned(16))) char smem[K3_LDS];
  const int tid = threadIdx.x, lane = tid & 63;
  const int wave = __builtin_amdgcn_readfirstlane(tid >> 6);
  STAMP(2, 0);
  const int xcd = blockIdx.x & 7, idx = blockIdx.x >> 3;
  const int tb = (xcd & 3) * 8 + (idx & 7), fb = (xcd >> 2) * 4 + (idx >> 3);
  const int T0 = tb * 64;
  const int r = lane & 15, q = lane >> 4;
  constexpr int NT = 16;
  const int rot = 2 * (idx & 7) + 4 * (idx >> 3);
#define KT(i) (((i) + rot) & 15)

  f32x4 ps[4][2];
#pragma unroll
  for (int pp = 0; pp < 4; ++pp)
#pragma unroll
    for (int j = 0; j < 2; ++j) ps[pp][j] = *(const f32x4*)(pstat + (size_t)pp * S_ + tid * 4 + 2 * j);

  auto stats = [&]() {
    float rr[4]; unsigned mm[2];
#pragma unroll
    for (int j = 0; j < 2; ++j) {
      float mn[2];
#pragma unroll
      for (int e = 0; e < 2; ++e) {
        const float a0 = ps[0][j][2 * e], a1 = ps[1][j][2 * e], a2 = ps[2][j][2 * e], a3 = ps[3][j][2 * e];
        const float mean = (128.0f * a0 + 256.0f * (a1 + a2 + a3)) * (1.0f / FH_);
        const float d0 = a0 - mean, d1 = a1 - mean, d2 = a2 - mean, d3 = a3 - mean;
        const float M2 = ps[0][j][2 * e + 1] + ps[1][j][2 * e + 1] + ps[2][j][2 * e + 1] + ps[3][j][2 * e + 1] +
                         128.0f * d0 * d0 + 256.0f * (d1 * d1 + d2 * d2 + d3 * d3);
        const float var = rbf(M2 * (1.0f / FH_));
        rr[2 * j + e] = 1.0f / rbf(sqrtf(rbf(var + 1e-5f)));
        mn[e] = mean;
      }
      mm[j] = pack2(mn[0], mn[1]);
    }
    *(f32x4*)(smem + K3_RT + tid * 16) = (f32x4){rr[0], rr[1], rr[2], rr[3]};
    *(u32x2*)(smem + K3_MT + tid * 8) = (u32x2){mm[0], mm[1]};
  };

  if (wave >= 4) {
    const int pt = tid - 256;
    const u16* wsrc = w_sp + (size_t)(T0 + (pt >> 4)) * S_ + (pt & 15) * 8;
    u32x4 wreg[K3_D][4];
#pragma unroll
    for (int d = 0; d < K3_D; ++d)
#pragma unroll
      for (int j = 0; j < 4; ++j) wreg[d][j] = *(const u32x4*)(wsrc + (size_t)(16 * j) * S_ + KT(d) * 128);
    stats();
    __syncthreads();
    auto stageW = [&](const u32x4 (&wr)[4], int kt, int buf) {
      const float* rt = (const float*)(smem + K3_RT) + kt * 128 + (pt & 15) * 8;
      const f32x4 ra = *(const f32x4*)rt, rb4 = *(const f32x4*)(rt + 4);
#pragma unroll
      for (int j = 0; j < 4; ++j) {
        const int row = (pt >> 4) + 16 * j;
        u32x4 o;
        o[0] = pack2(lo2f(wr[j][0]) * ra[0], hi2f(wr[j][0]) * ra[1]);
        o[1] = pack2(lo2f(wr[j][1]) * ra[2], hi2f(wr[j][1]) * ra[3]);
        o[2] = pack2(lo2f(wr[j][2]) * rb4[0], hi2f(wr[j][2]) * rb4[1]);
        o[3] = pack2(lo2f(wr[j][3]) * rb4[2], hi2f(wr[j][3]) * rb4[3]);
        *(u32x4*)(smem + K3_WB + buf * 16384 + row * 256 + (((pt & 15) ^ (row & 15)) * 16)) = o;
      }
    };
#pragma unroll
    for (int it = 0; it < NT; ++it) {
      const int d = it % K3_D;
      stageW(wreg[d], KT(it), it % 3);
      if (it + K3_D < NT) {
#pragma unroll
        for (int j = 0; j < 4; ++j)
          wreg[d][j] = *(const u32x4*)(wsrc + (size_t)(16 * j) * S_ + KT(it + K3_D) * 128);
      }
      if (it >= 1) __syncthreads();
    }
    __syncthreads();
    __syncthreads();
    __syncthreads();
    return;
  }

  const int ft0 = fb * 7 + 2 * wave, ft1 = fb * 7 + (wave < 3 ? 2 * wave + 1 : 2 * wave);
  const u16* zsrc0 = z2T + (size_t)(ft0 * 64 * 64 + lane) * 8;
  const u16* zsrc1 = z2T + (size_t)(ft1 * 64 * 64 + lane) * 8;
  bf16x8 zreg[K3_DZ][2][4];
#pragma unroll
  for (int d = 0; d < K3_DZ; ++d)
#pragma unroll
    for (int ks = 0; ks < 4; ++ks) {
      zreg[d][0][ks] = *(const bf16x8*)(zsrc0 + (KT(d) * 4 + ks) * 512);
      zreg[d][1][ks] = *(const bf16x8*)(zsrc1 + (KT(d) * 4 + ks) * 512);
    }
  stats();
  STAMP(2, 1);
  __syncthreads();
  STAMP(2, 2);
  __syncthreads();
  STAMP(2, 3);

  f32x4 acc[2][4];
#pragma unroll
  for (int i = 0; i < 2; ++i)
#pragma unroll
    for (int n = 0; n < 4; ++n) acc[i][n] = (f32x4){0.f, 0.f, 0.f, 0.f};
  u32x2 zz1[2][4];
  float bsp[4];
  auto ldfrag = [&](bf16x8 (&bf)[4], int it, int ks) {
#pragma unroll
    for (int n = 0; n < 4; ++n)
      bf[n] = *(const bf16x8*)(smem + K3_WB + (it % 3) * 16384 + (16 * n + r) * 256 + (((4 * ks + q) ^ r) * 16));
  };
  bf16x8 bfa[4], bfb[4];
  ldfrag(bfa, 0, 0);
#pragma unroll
  for (int it = 0; it < NT; ++it) {
    const int d = it % K3_DZ;
    if (wave == 3) {
#pragma unroll
      for (int ks = 0; ks < 4; ++ks)
        zreg[d][1][ks] = *(const bf16x8*)(smem + K3_MT + (KT(it) * 128 + 32 * ks + 8 * q) * 2);
    }
#pragma unroll
    for (int ks = 0; ks < 4; ks += 2) {
      ldfrag(bfb, it, ks + 1);
      __builtin_amdgcn_sched_barrier(0);
#pragma unroll
      for (int n = 0; n < 4; ++n) {
        acc[0][n] = mfma16(zreg[d][0][ks], bfa[n], acc[0][n]);
        acc[1][n] = mfma16(zreg[d][1][ks], bfa[n], acc[1][n]);
      }
      __builtin_amdgcn_sched_barrier(0);
      if (ks == 0) ldfrag(bfa, it, 2);
      else if (it + 1 < NT) ldfrag(bfa, it + 1, 0);
      __builtin_amdgcn_sched_barrier(0);
#pragma unroll
      for (int n = 0; n < 4; ++n) {
        acc[0][n] = mfma16(zreg[d][0][ks + 1], bfb[n], acc[0][n]);
        acc[1][n] = mfma16(zreg[d][1][ks + 1], bfb[n], acc[1][n]);
      }
      __builtin_amdgcn_sched_barrier(0);
    }
    if (it + K3_DZ < NT) {
#pragma unroll
      for (int ks = 0; ks < 4; ++ks) {
        zreg[d][0][ks] = *(const bf16x8*)(zsrc0 + (KT(it + K3_DZ) * 4 + ks) * 512);
        zreg[d][1][ks] = *(const bf16x8*)(zsrc1 + (KT(it + K3_DZ) * 4 + ks) * 512);
      }
    }
    if (it == NT - K3_DZ) {
#pragma unroll
      for (int n = 0; n < 4; ++n) {
        bsp[n] = bf2f(b_sp[T0 + 16 * n + r]);
        zz1[0][n] = *(const u32x2*)(z1 + (size_t)(((tb * 4 + n) * 56 + ft0) * 64 + lane) * 4);
        zz1[1][n] = *(const u32x2*)(z1 + (size_t)(((tb * 4 + n) * 56 + ft1) * 64 + lane) * 4);
      }
    }
    __syncthreads();
  }
  float* cbuf = (float*)(smem + K3_CB);
  if (wave == 3 && q == 0) {
#pragma unroll
    for (int n = 0; n < 4; ++n) cbuf[16 * n + r] = acc[1][n][0];
  }
  STAMP(2, 4);
  __syncthreads();
#pragma unroll
  for (int n = 0; n < 4; ++n) {
    const float c = cbuf[16 * n + r];
    const float bb = bsp[n];
#pragma unroll
    for (int i = 0; i < 2; ++i) {
      if (i == 1 && wave == 3) continue;
      const int ftile = i ? ft1 : ft0;
      const u32x2 zz = zz1[i][n];
      float g[4];
#pragma unroll
      for (int j = 0; j < 4; ++j) g[j] = rbf(rbf(acc[i][n][j] - c) + bb);
      u32x2 o = {pack2(lo2f(zz[0]) * g[0], hi2f(zz[0]) * g[1]), pack2(lo2f(zz[1]) * g[2], hi2f(zz[1]) * g[3])};
      *(u32x2*)(uout + (size_t)((((tb * 4 + n) * 28 + (ftile >> 1)) * 64 + 16 * (2 * (ftile & 1) + (q >> 1)) + r) * 8 + 4 * (q & 1))) = o;
    }
  }
  STAMP(2, 5);
}

constexpr int K4_LDS = 64 * 1792;

__global__ __launch_bounds__(256) void k4_gemm3(
    const u16* __restrict__ uin, const u16* __restrict__ w_outT, const u16* __restrict__ hin,
    u16* __restrict__ out) {
  __shared__ __attribute__((aligned(16))) char smem[K4_LDS];
  const int tid = threadIdx.x, lane = tid & 63, wave = tid >> 6;
  STAMP(3, 0);
  const int xcd = blockIdx.x & 7, idx = blockIdx.x >> 3;
  const int tb = xcd * 4 + (idx & 3), db = idx >> 2;
  const int T0 = tb * 64, D0 = db * 64;
  const int r = lane & 15, q = lane >> 4;

#pragma unroll
  for (int h = 0; h < 2; ++h) {
    u32x4 ul[14];
#pragma unroll
    for (int j = 0; j < 14; ++j)
      ul[j] = *(const u32x4*)(uin + (size_t)tb * 64 * FH_ + (size_t)(tid + 256 * (14 * h + j)) * 8);
#pragma unroll
    for (int j = 0; j < 14; ++j) *(u32x4*)(smem + (tid + 256 * (14 * h + j)) * 16) = ul[j];
  }
  bf16x8 wf[28];
#pragma unroll
  for (int ks = 0; ks < 28; ++ks)
    wf[ks] = *(const bf16x8*)(w_outT + (size_t)(((db * 4 + wave) * 28 + ks) * 64 + lane) * 8);
  STAMP(3, 1);
  __syncthreads();
  STAMP(3, 2);
  f32x4 acc[4];
#pragma unroll
  for (int n = 0; n < 4; ++n) acc[n] = (f32x4){0.f, 0.f, 0.f, 0.f};
#pragma unroll
  for (int ks = 0; ks < 28; ++ks) {
#pragma unroll
    for (int n = 0; n < 4; ++n) {
      const bf16x8 b = *(const bf16x8*)(smem + ((n * 28 + ks) * 64 + lane) * 16);
      acc[n] = mfma16(wf[ks], b, acc[n]);
    }
  }
#pragma unroll
  for (int n = 0; n < 4; ++n) {
    const int t = T0 + 16 * n + r, d = D0 + 16 * wave + 4 * q;
    const u32x2 hh = *(const u32x2*)(hin + (size_t)t * D_ + d);
    u32x2 o = {pack2(lo2f(hh[0]) + rbf(acc[n][0]), hi2f(hh[0]) + rbf(acc[n][1])),
               pack2(lo2f(hh[1]) + rbf(acc[n][2]), hi2f(hh[1]) + rbf(acc[n][3]))};
    *(u32x2*)(out + (size_t)t * D_ + d) = o;
  }
  STAMP(3, 3);
}

extern "C" void kernel_launch(void* const* d_in, const int* in_sizes, int n_in, void* d_out,
                              int out_size, void* d_ws, size_t ws_size, hipStream_t stream) {
  const u16* xq = (const u16*)d_in[0];
  const u16* kin = (const u16*)d_in[1];
  const u16* vin = (const u16*)d_in[2];
  const u16* shk = (const u16*)d_in[3];
  const u16* ssmk = (const u16*)d_in[4];
  const u16* ssmD = (const u16*)d_in[5];
  const u16* w_in = (const u16*)d_in[7];
  const u16* w_sp = (const u16*)d_in[8];
  const u16* b_sp = (const u16*)d_in[9];
  const u16* w_out = (const u16*)d_in[10];
  char* ws = (char*)d_ws;
  u16* yT = (u16*)(ws + WS_YT);
  u16* h = (u16*)(ws + WS_H);
  u16* w_inT = (u16*)(ws + WS_WINT);
  u16* w_outT = (u16*)(ws + WS_WOUTT);
  float2* ln1 = (float2*)(ws + WS_LN1);
  u16* z1 = (u16*)(ws + WS_Z1);
  u16* z2T = (u16*)(ws + WS_Z2T);
  float2* pst = (float2*)(ws + WS_PST);
  u16* u = (u16*)(ws + WS_U);

  k1_conv<<<256, 512, 0, stream>>>(kin, vin, shk, ssmk, ssmD, yT, xq, w_in, w_out, w_inT, w_outT, ln1);
  k2_gemm1<<<224, 512, 0, stream>>>(xq, yT, ln1, w_inT, h, z1, z2T, pst);
  k3_gemm2<<<256, 512, 0, stream>>>(w_sp, b_sp, z2T, z1, pst, u);
  k4_gemm3<<<224, 256, 0, stream>>>(u, w_outT, h, (u16*)d_out);
}
```

```cpp
#include <hip/hip_runtime.h>
#include <hip/hip_bf16.h>
#include <stdint.h>

typedef __attribute__((ext_vector_type(8))) short bf16x8;
typedef __attribute__((ext_vector_type(4))) short s16x4;
typedef __attribute__((ext_vector_type(4))) float f32x4;
typedef __attribute__((ext_vector_type(2))) float f32x2;
typedef __attribute__((ext_vector_type(4))) unsigned int u32x4;
typedef __attribute__((ext_vector_type(2))) unsigned int u32x2;
typedef __attribute__((ext_vector_type(2))) __bf16 bf16x2_t;
typedef unsigned short u16;

#define DEVI __device__ __forceinline__
#define STAMP(kid, i)

constexpr int S_ = 2048, D_ = 448, F_ = 1792, FH_ = 896;

DEVI float lo2f(unsigned v) { return __uint_as_float(v << 16); }
DEVI float hi2f(unsigned v) { return __uint_as_float(v & 0xffff0000u); }
DEVI float bf2f(u16 v) { return __uint_as_float(((unsigned)v) << 16); }
DEVI unsigned pack2(float a, float b) {
  f32x2 f = {a, b};
  bf16x2_t h = __builtin_convertvector(f, bf16x2_t);
  return __builtin_bit_cast(unsigned, h);
}
DEVI float rbf(float x) { return lo2f(pack2(x, x)); }
DEVI f32x4 mfma16(bf16x8 a, bf16x8 b, f32x4 c) {
  return __builtin_amdgcn_mfma_f32_16x16x32_bf16(a, b, c, 0, 0, 0);
}
template <int CTRL> DEVI float dpp_f(float x) {
  return __builtin_bit_cast(float, __builtin_amdgcn_update_dpp(0, __builtin_bit_cast(int, x), CTRL, 0xf, 0xf, false));
}
DEVI float row16_sum(float x) {
  x += dpp_f<0x121>(x);
  x += dpp_f<0x122>(x);
  x += dpp_f<0x124>(x);
  x += dpp_f<0x128>(x);
  return x;
}
DEVI float gelu_tanh(float x) {
  const float u = x * (0.7978845608028654f + 0.035677408136300125f * x * x);
  const float e = __builtin_amdgcn_exp2f(-2.885390081777927f * u);
  return x * __builtin_amdgcn_rcpf(1.0f + e);
}

constexpr size_t WS_YT = 0;
constexpr size_t WS_H = WS_YT + (size_t)D_ * S_ * 2;
constexpr size_t WS_WINT = WS_H + (size_t)S_ * D_ * 2;
constexpr size_t WS_WOUTT = WS_WINT + (size_t)F_ * D_ * 2;
constexpr size_t WS_LN1 = WS_WOUTT + (size_t)D_ * FH_ * 2;
constexpr size_t WS_Z1 = WS_LN1 + (size_t)S_ * 8;
constexpr size_t WS_Z2T = WS_Z1 + (size_t)S_ * FH_ * 2;
constexpr size_t WS_PST = WS_Z2T + (size_t)S_ * FH_ * 2;
constexpr size_t WS_U = WS_PST + (size_t)8 * S_ * 8;
constexpr size_t WS_CNT = WS_U + (size_t)S_ * FH_ * 2;

constexpr int K1_XREV = 0;
constexpr int K1_XSTRIDE = 2304 * 2;
constexpr int K1_COP = 9216;
constexpr int K1_CSTRIDE = 4128;
constexpr int K1_RED = K1_COP + 2 * 8 * K1_CSTRIDE;
constexpr int K1_LDS = K1_RED + 8 * 8192;

__global__ __launch_bounds__(512) void k1_conv(
    const u16* __restrict__ kin, const u16* __restrict__ vin, const u16* __restrict__ shk,
    const u16* __restrict__ ssmk, const u16* __restrict__ ssmD, u16* __restrict__ yT,
    const u16* __restrict__ xin, const u16* __restrict__ w_in, const u16* __restrict__ w_out,
    u16* __restrict__ w_inT, u16* __restrict__ w_outT, float2* __restrict__ ln1,
    unsigned* __restrict__ cnt) {
  __shared__ __attribute__((aligned(16))) char smem[K1_LDS];
  const int tid = threadIdx.x, lane = tid & 63, wave = tid >> 6;
  STAMP(0, 0);
  const int xcd = blockIdx.x & 7, idx = blockIdx.x >> 3;

  if (idx >= 28) {
    const int a = xcd * 4 + (idx - 28);
    ((unsigned long long*)cnt)[a * 512 + tid] = 0ull;
    {
      u32x4 xr[8];
#pragma unroll
      for (int rr = 0; rr < 8; ++rr) {
        xr[rr] = (u32x4){0u, 0u, 0u, 0u};
        if (lane < 56) xr[rr] = *(const u32x4*)(xin + (size_t)(a * 64 + wave * 8 + rr) * D_ + lane * 8);
      }
#pragma unroll
      for (int rr = 0; rr < 8; ++rr) {
        float v[8];
#pragma unroll
        for (int j = 0; j < 4; ++j) { v[2 * j] = lo2f(xr[rr][j]); v[2 * j + 1] = hi2f(xr[rr][j]); }
        float sm = 0.f;
#pragma unroll
        for (int j = 0; j < 8; ++j) sm += v[j];
#pragma unroll
        for (int o = 1; o < 64; o <<= 1) sm += __shfl_xor(sm, o);
        const float mean = sm * (1.0f / D_);
        float m2 = 0.f;
        if (lane < 56) {
#pragma unroll
          for (int j = 0; j < 8; ++j) { float d = v[j] - mean; m2 += d * d; }
        }
#pragma unroll
        for (int o = 1; o < 64; o <<= 1) m2 += __shfl_xor(m2, o);
        if (lane == 0) {
          const float var = rbf(m2 * (1.0f / D_));
          const float sd = rbf(sqrtf(rbf(var + 1e-5f)));
          ln1[a * 64 + wave * 8 + rr] = make_float2(rbf(mean), 1.0f / sd);
        }
      }
    }
    {
      typedef __attribute__((address_space(3))) s16x4* lp;
      char* tl = smem + wave * 4608;
      const int aw = a * 8 + wave;
      const int r = lane & 15, q = lane >> 4;
      u32x4 sl[3][4];
#pragma unroll
      for (int j = 0; j < 3; ++j) {
        const int id = aw + 256 * j;
        if (id < 588) {
          const u16* src; int ld;
          if (id < 392) { src = w_in + (size_t)(id / 28) * 32 * F_ + (id % 28) * 64; ld = F_; }
          else { const int i2 = id - 392; src = w_out + (size_t)(i2 / 7) * 32 * D_ + (i2 % 7) * 64; ld = D_; }
#pragma unroll
          for (int i = 0; i < 4; ++i)
            sl[j][i] = *(const u32x4*)(src + (size_t)(8 * i + (lane >> 3)) * ld + (lane & 7) * 8);
        }
      }
#pragma unroll
      for (int j = 0; j < 3; ++j) {
        const int id = aw + 256 * j;
        if (id < 588) {
          u16* dst;
          int bstride;
          if (id < 392) { dst = w_inT + (size_t)((id % 28) * 4 * 14 + (id / 28)) * 512; bstride = 14 * 512; }
          else { const int i2 = id - 392; dst = w_outT + (size_t)((i2 % 7) * 4 * 28 + (i2 / 7)) * 512; bstride = 28 * 512; }
#pragma unroll
          for (int i = 0; i < 4; ++i)
            *(u32x4*)(tl + (8 * i + (lane >> 3)) * 144 + (lane & 7) * 16) = sl[j][i];
#pragma unroll
          for (int n = 0; n < 4; ++n) {
            const char* a0p = tl + (8 * q + (r >> 2)) * 144 + (16 * n + 4 * (r & 3)) * 2;
            s16x4 v0 = __builtin_amdgcn_ds_read_tr16_b64_v4i16((lp)(a0p));
            s16x4 v1 = __builtin_amdgcn_ds_read_tr16_b64_v4i16((lp)(a0p + 4 * 144));
            bf16x8 o = {v0[0], v0[1], v0[2], v0[3], v1[0], v1[1], v1[2], v1[3]};
            *(bf16x8*)(dst + (size_t)n * bstride + lane * 8) = o;
          }
        }
      }
    }
    return;
  }

  const int d0 = (xcd * 28 + idx) * 2;
  const int s0 = tid * 4;
  unsigned kk[4], vv[4];
#pragma unroll
  for (int j = 0; j < 4; ++j) kk[j] = *(const unsigned*)(kin + (size_t)(s0 + j) * D_ + d0);
#pragma unroll
  for (int j = 0; j < 4; ++j) vv[j] = *(const unsigned*)(vin + (size_t)(s0 + j) * D_ + d0);
  const int tch = tid >> 8, mm = tid & 255;
  const u16* krow = ssmk + (size_t)(d0 + tch) * S_;
  u32x4 wa = *(const u32x4*)(krow + mm * 8);
  u32x4 wb = {0u, 0u, 0u, 0u};
  if (mm < 255) wb = *(const u32x4*)(krow + mm * 8 + 8);
  unsigned hk[3] = {0u, 0u, 0u};
  if (lane == 0 && s0 > 0) {
#pragma unroll
    for (int j = 0; j < 3; ++j) hk[j] = *(const unsigned*)(kin + (size_t)(s0 - 3 + j) * D_ + d0);
  }
  const u32x4 skw = *(const u32x4*)(shk + d0 * 4);
  const unsigned dDw = *(const unsigned*)(ssmD + d0);

  if (tid < 64) {
    u32x4 z = {0u, 0u, 0u, 0u};
    *(u32x4*)(smem + K1_XREV + (tid >> 5) * K1_XSTRIDE + (2048 + (tid & 31) * 8) * 2) = z;
  }
  {
    unsigned w[8] = {wa[0], wa[1], wa[2], wa[3], wb[0], wb[1], wb[2], wb[3]};
    unsigned al[7];
#pragma unroll
    for (int i = 0; i < 7; ++i) al[i] = (w[i] >> 16) | (w[i + 1] << 16);
    char* base = smem + K1_COP + tch * 8 * K1_CSTRIDE + (mm + 2) * 16;
#pragma unroll
    for (int c = 0; c < 8; ++c) {
      u32x4 o;
      if ((c & 1) == 0) { o[0] = w[c / 2]; o[1] = w[c / 2 + 1]; o[2] = w[c / 2 + 2]; o[3] = w[c / 2 + 3]; }
      else { o[0] = al[c / 2]; o[1] = al[c / 2 + 1]; o[2] = al[c / 2 + 2]; o[3] = al[c / 2 + 3]; }
      *(u32x4*)(base + c * K1_CSTRIDE) = o;
    }
    if (mm < 2) {
      unsigned w2[8] = {0u, 0u, 0u, 0u, 0u, 0u, 0u, 0u};
      if (mm == 0) { w2[4] = wa[0]; w2[5] = wa[1]; w2[6] = wa[2]; w2[7] = wa[3]; }
      unsigned a2[7];
#pragma unroll
      for (int i = 0; i < 7; ++i) a2[i] = (w2[i] >> 16) | (w2[i + 1] << 16);
      char* b2 = smem + K1_COP + tch * 8 * K1_CSTRIDE + (1 - mm) * 16;
#pragma unroll
      for (int c = 0; c < 8; ++c) {
        u32x4 o;
        if ((c & 1) == 0) { o[0] = w2[c / 2]; o[1] = w2[c / 2 + 1]; o[2] = w2[c / 2 + 2]; o[3] = w2[c / 2 + 3]; }
        else { o[0] = a2[c / 2]; o[1] = a2[c / 2 + 1]; o[2] = a2[c / 2 + 2]; o[3] = a2[c / 2 + 3]; }
        *(u32x4*)(b2 + c * K1_CSTRIDE) = o;
      }
    }
  }
  {
    unsigned p1 = __shfl_up(kk[3], 1), p2 = __shfl_up(kk[2], 1), p3 = __shfl_up(kk[1], 1);
    if (lane == 0) { p3 = hk[0]; p2 = hk[1]; p1 = hk[2]; }
    unsigned kw[7] = {p3, p2, p1, kk[0], kk[1], kk[2], kk[3]};
#pragma unroll
    for (int ch = 0; ch < 2; ++ch) {
      const float sk0 = lo2f(skw[2 * ch]), sk1 = hi2f(skw[2 * ch]);
      const float sk2 = lo2f(skw[2 * ch + 1]), sk3 = hi2f(skw[2 * ch + 1]);
      float kf[7];
#pragma unroll
      for (int j = 0; j < 7; ++j) kf[j] = ch ? hi2f(kw[j]) : lo2f(kw[j]);
      float kvv[4];
#pragma unroll
      for (int j = 0; j < 4; ++j) {
        float a = rbf(kf[j] * sk3);
        a = rbf(a + rbf(kf[j + 1] * sk2));
        a = rbf(a + rbf(kf[j + 2] * sk1));
        a = rbf(a + rbf(kf[j + 3] * sk0));
        const float vf = ch ? hi2f(vv[j]) : lo2f(vv[j]);
        kvv[j] = a * vf;
      }
      u32x2 o = {pack2(kvv[3], kvv[2]), pack2(kvv[1], kvv[0])};
      *(u32x2*)(smem + K1_XREV + ch * K1_XSTRIDE + (2044 - s0) * 2) = o;
    }
  }
  STAMP(0, 1);
  __syncthreads();
  STAMP(0, 2);

  {
    const int ch = wave & 1, r = lane & 15, q = lane >> 4;
    f32x4 acc[8];
#pragma unroll
    for (int t = 0; t < 8; ++t) acc[t] = (f32x4){0.f, 0.f, 0.f, 0.f};
    const char* abase = smem + K1_COP + ch * 8 * K1_CSTRIDE + ((r + 1) & 7) * K1_CSTRIDE +
                        (2 * (q >> 1) - (q & 1) + ((r + 9) >> 3)) * 16;
    const char* bbase = smem + K1_XREV + ch * K1_XSTRIDE + (2040 - 16 * r + 16 * (q >> 1) - 8 * (q & 1)) * 2;
#pragma unroll
    for (int cc = 0; cc < 2; ++cc) {
      const int rho = 2 * (wave >> 1) + cc;
      bf16x8 A[8], B[8];
#pragma unroll
      for (int i = 0; i < 8; ++i) A[i] = *(const bf16x8*)(abase + (rho + 8 * i) * 64);
#pragma unroll
      for (int i = 0; i < 8; ++i) B[i] = *(const bf16x8*)(bbase - (8 * i - rho) * 64);
#pragma unroll
      for (int t = 0; t < 8; ++t)
#pragma unroll
        for (int i = 0; i <= t; ++i) acc[t] = mfma16(A[i], B[t - i], acc[t]);
    }
    float* red = (float*)(smem + K1_RED + wave * 8192);
#pragma unroll
    for (int t = 0; t < 8; ++t) *(f32x4*)(red + 256 * t + 16 * r + 4 * q) = acc[t];
  }
  STAMP(0, 3);
  __syncthreads();
  STAMP(0, 4);
  {
    const int ch = tid >> 8, e0 = (tid & 255) * 8;
    f32x4 sa = {0.f, 0.f, 0.f, 0.f}, sb = {0.f, 0.f, 0.f, 0.f};
#pragma unroll
    for (int w = 0; w < 4; ++w) {
      const float* red = (const float*)(smem + K1_RED + (2 * w + ch) * 8192);
      sa += *(const f32x4*)(red + e0);
      sb += *(const f32x4*)(red + e0 + 4);
    }
    const u32x4 kr = *(const u32x4*)(smem + K1_XREV + ch * K1_XSTRIDE + (2040 - e0) * 2);
    const float Dv = ch ? hi2f(dDw) : lo2f(dDw);
    float kvf[8];
    kvf[7] = lo2f(kr[0]); kvf[6] = hi2f(kr[0]); kvf[5] = lo2f(kr[1]); kvf[4] = hi2f(kr[1]);
    kvf[3] = lo2f(kr[2]); kvf[2] = hi2f(kr[2]); kvf[1] = lo2f(kr[3]); kvf[0] = hi2f(kr[3]);
    float y[8];
#pragma unroll
    for (int j = 0; j < 4; ++j) {
      y[j] = rbf(sa[j]) + rbf(kvf[j] * Dv);
      y[j + 4] = rbf(sb[j]) + rbf(kvf[j + 4] * Dv);
    }
    u32x4 o = {pack2(y[0], y[1]), pack2(y[2], y[3]), pack2(y[4], y[5]), pack2(y[6], y[7])};
    *(u32x4*)(yT + (size_t)(d0 + ch) * S_ + e0) = o;
  }
  STAMP(0, 5);
}

constexpr int K2_YT = 0;
constexpr int K2_AT = 448 * 80;
constexpr int K2_ST = K2_AT + 32 * 1024;
constexpr int K2_FS = K2_ST + 8 * 32 * 8;
constexpr int K2_LDS = K2_FS + 32 * 8;

DEVI f32x2 unp(unsigned v) { return (f32x2){lo2f(v), hi2f(v)}; }
DEVI unsigned pk(f32x2 v) { return pack2(v[0], v[1]); }

__global__ __launch_bounds__(512) void k2_gemm1(
    const u16* __restrict__ xin, const u16* __restrict__ yT, const float2* __restrict__ ln1,
    const u16* __restrict__ w_inT, u16* __restrict__ hout, u16* __restrict__ z1,
    u16* __restrict__ z2T, unsigned long long* pstat, unsigned* cnt) {
  __shared__ __attribute__((aligned(16))) char smem[K2_LDS];
  const int tid = threadIdx.x, lane = tid & 63;
  const int wave = __builtin_amdgcn_readfirstlane(tid >> 6);
  STAMP(1, 0);
  const int xcd = blockIdx.x & 7, idx = blockIdx.x >> 3;
  const int rb = xcd * 8 + (idx & 7), cb = idx >> 3;
  const int R0 = rb * 32;
  const int r = lane & 15, q = lane >> 4;

  u32x4 yl[4], xg[4];
#pragma unroll
  for (int j = 0; j < 4; ++j) {
    const int id = tid + 512 * j;
    if (j < 3 || tid < 256) {
      yl[j] = *(const u32x4*)(yT + (size_t)(id >> 2) * S_ + R0 + (id & 3) * 8);
      xg[j] = *(const u32x4*)(xin + (size_t)R0 * D_ + id * 8);
    }
  }
  const int prow = 16 * (wave & 1) + r;
  const int dq0 = (wave >> 1) * 28 + q;
  const float2 l1 = ln1[R0 + prow];
  const int wv = wave < 7 ? wave : 6;
  const u16* wz2 = w_inT + (size_t)((56 + cb * 14 + 2 * wv) * 14 * 64 + lane) * 8;
  const u16* wz1 = w_inT + (size_t)((cb * 14 + 2 * wv) * 14 * 64 + lane) * 8;
  bf16x8 wf[2][14];
#pragma unroll
  for (int ks = 0; ks < 7; ++ks)
#pragma unroll
    for (int i = 0; i < 2; ++i) wf[i][ks] = *(const bf16x8*)(wz2 + (i * 14 + ks) * 512);
#pragma unroll
  for (int j = 0; j < 4; ++j) {
    const int id = tid + 512 * j;
    if (j < 3 || tid < 256) {
      *(u32x4*)(smem + K2_YT + (id >> 2) * 80 + (id & 3) * 16) = yl[j];
      const int row = id / 56, c = id % 56;
      *(u32x4*)(smem + K2_AT + row * 1024 + ((c ^ (row & 15)) * 16)) = xg[j];
    }
  }
  STAMP(1, 1);
  __syncthreads();
  STAMP(1, 2);
  f32x2 hv[7][2];
  f32x2 sum2 = {0.f, 0.f};
  {
    typedef __attribute__((address_space(3))) s16x4* lp;
    const int qq = r >> 2, pp = r & 3;
    const f32x2 m1 = {l1.x, l1.x}, r1 = {l1.y, l1.y};
#pragma unroll
    for (int it = 0; it < 7; ++it) {
      const int dq = dq0 + 4 * it;
      const char* a0 = smem + K2_YT + (4 * dq + qq) * 80 + (16 * (wave & 1) + 4 * pp) * 2;
      const u32x2 y0 = __builtin_bit_cast(u32x2, __builtin_amdgcn_ds_read_tr16_b64_v4i16((lp)(a0)));
      const u32x2 xv = *(const u32x2*)(smem + K2_AT + prow * 1024 + (((dq >> 1) ^ r) * 16) + (dq & 1) * 8);
#pragma unroll
      for (int j = 0; j < 2; ++j) {
        const f32x2 x2 = unp(xv[j]);
        const unsigned dd = pk(x2 - m1);
        const unsigned qn = pk(unp(dd) * r1);
        const unsigned aa = pk(unp(qn) * unp(y0[j]));
        hv[it][j] = unp(pk(x2 + unp(aa)));
        sum2 += hv[it][j];
      }
#pragma unroll
      for (int i = 0; i < 2; ++i) wf[i][7 + it] = *(const bf16x8*)(wz2 + (i * 14 + 7 + it) * 512);
      __builtin_amdgcn_sched_barrier(0);
    }
  }
  float sum = sum2[0] + sum2[1];
  sum += __shfl_xor(sum, 16);
  sum += __shfl_xor(sum, 32);
  const float mh = sum * (1.0f / 112.0f);
  f32x2 q2 = {0.f, 0.f};
  {
    const f32x2 mh2 = {mh, mh};
#pragma unroll
    for (int it = 0; it < 7; ++it)
#pragma unroll
      for (int j = 0; j < 2; ++j) {
        const f32x2 d = hv[it][j] - mh2;
        q2 += d * d;
      }
  }
  float m2 = q2[0] + q2[1];
  m2 += __shfl_xor(m2, 16);
  m2 += __shfl_xor(m2, 32);
  float2* st = (float2*)(smem + K2_ST);
  if (q == 0) st[wave * 16 + r] = make_float2(mh, m2);
  STAMP(1, 3);
  __syncthreads();
  STAMP(1, 4);
  {
    float2 pq[4];
    float ms = 0.f;
#pragma unroll
    for (int w = 0; w < 4; ++w) { pq[w] = st[((wave & 1) + 2 * w) * 16 + r]; ms += pq[w].x; }
    const float mean = 0.25f * ms;
    float M2 = 0.f;
#pragma unroll
    for (int w = 0; w < 4; ++w) { const float dl = pq[w].x - mean; M2 += pq[w].y + 112.0f * dl * dl; }
    const float var = rbf(M2 * (1.0f / D_));
    const float mb = rbf(mean);
    const float rinv = 1.0f / rbf(sqrtf(rbf(var + 1e-5f)));
    const f32x2 mb2 = {mb, mb}, ri2 = {rinv, rinv};
#pragma unroll
    for (int it = 0; it < 7; ++it) {
      u32x2 o2;
#pragma unroll
      for (int j = 0; j < 2; ++j) o2[j] = pk(unp(pk(hv[it][j] - mb2)) * ri2);
      const int dq = dq0 + 4 * it;
      *(u32x2*)(smem + K2_AT + prow * 1024 + (((dq >> 1) ^ r) * 16) + (dq & 1) * 8) = o2;
      if (cb == 0) {
        u32x2 h2 = {pk(hv[it][0]), pk(hv[it][1])};
        *(u32x2*)(hout + (size_t)(R0 + prow) * D_ + dq * 4) = h2;
      }
    }
  }
  STAMP(1, 5);
  __syncthreads();
  STAMP(1, 6);

  u32x2 zp[2][2];
  bf16x8 wg[2][14];
  if (wave < 7) {
    f32x4 acc[2][2];
#pragma unroll
    for (int i = 0; i < 2; ++i)
#pragma unroll
      for (int s = 0; s < 2; ++s) acc[i][s] = (f32x4){0.f, 0.f, 0.f, 0.f};
#pragma unroll
    for (int ks = 0; ks < 14; ++ks) {
      bf16x8 xf[2];
#pragma unroll
      for (int s = 0; s < 2; ++s)
        xf[s] = *(const bf16x8*)(smem + K2_AT + (16 * s + r) * 1024 + (((4 * ks + q) ^ r) * 16));
#pragma unroll
      for (int i = 0; i < 2; ++i)
#pragma unroll
        for (int s = 0; s < 2; ++s) acc[i][s] = mfma16(xf[s], wf[i][ks], acc[i][s]);
#pragma unroll
      for (int i = 0; i < 2; ++i) wg[i][ks] = *(const bf16x8*)(wz1 + (i * 14 + ks) * 512);
    }
#pragma unroll
    for (int s = 0; s < 2; ++s) {
#pragma unroll
      for (int i = 0; i < 2; ++i) {
        float zz[4];
#pragma unroll
        for (int j = 0; j < 4; ++j) zz[j] = gelu_tanh(rbf(acc[i][s][j]));
        zp[i][s] = (u32x2){pack2(zz[0], zz[1]), pack2(zz[2], zz[3])};
      }
      float mw[4], qw[4];
#pragma unroll
      for (int j = 0; j < 4; ++j) {
        const float v0 = (j & 1) ? hi2f(zp[0][s][j >> 1]) : lo2f(zp[0][s][j >> 1]);
        const float v1 = (j & 1) ? hi2f(zp[1][s][j >> 1]) : lo2f(zp[1][s][j >> 1]);
        mw[j] = row16_sum(v0 + v1) * (1.0f / 32.0f);
        const float a0 = v0 - mw[j], a1 = v1 - mw[j];
        qw[j] = row16_sum(a0 * a0 + a1 * a1);
      }
      if (r == 0) {
#pragma unroll
        for (int j = 0; j < 4; ++j) st[wave * 32 + 16 * s + 4 * q + j] = make_float2(mw[j], qw[j]);
      }
    }
  }
  __syncthreads();
  if (wave == 7) {
    float2* fs = (float2*)(smem + K2_FS);
    const int row = lane >> 1;
    float msum = 0.f;
    float2 pw[7];
#pragma unroll
    for (int w = 0; w < 7; ++w) { pw[w] = st[w * 32 + row]; msum += pw[w].x; }
    const float bmean = msum * (1.0f / 7.0f);
    float bM2 = 0.f;
#pragma unroll
    for (int w = 0; w < 7; ++w) { const float dl = pw[w].x - bmean; bM2 += pw[w].y + 32.0f * dl * dl; }
    const float mine = (lane & 1) ? bM2 : bmean;
    unsigned long long* g = pstat + (size_t)rb * 256 + lane;
    __hip_atomic_store(g + cb * 64, (1ull << 32) | __float_as_uint(mine), __ATOMIC_RELAXED, __HIP_MEMORY_SCOPE_AGENT);
    float pv[4];
    for (int spin = 0; spin < (1 << 22); ++spin) {
      bool ok = true;
#pragma unroll
      for (int c = 0; c < 4; ++c) {
        const unsigned long long v = __hip_atomic_load(g + c * 64, __ATOMIC_RELAXED, __HIP_MEMORY_SCOPE_AGENT);
        pv[c] = __uint_as_float((unsigned)v);
        ok &= (v >> 32) == 1ull;
      }
      if (__all(ok)) break;
      __builtin_amdgcn_s_sleep(1);
    }
    float pm[4], pq[4];
#pragma unroll
    for (int c = 0; c < 4; ++c) {
      const float o = __shfl_xor(pv[c], 1);
      pm[c] = (lane & 1) ? o : pv[c];
      pq[c] = (lane & 1) ? pv[c] : o;
    }
    const float mean = (pm[0] + pm[1] + pm[2] + pm[3]) * 0.25f;
    float M2 = 0.f;
#pragma unroll
    for (int c = 0; c < 4; ++c) { const float dl = pm[c] - mean; M2 += pq[c] + 224.0f * dl * dl; }
    const float var = rbf(M2 * (1.0f / FH_));
    if ((lane & 1) == 0) fs[row] = make_float2(rbf(mean), 1.0f / rbf(sqrtf(rbf(var + 1e-5f))));
  } else {
    STAMP(1, 7);
    f32x4 acc[2][2];
#pragma unroll
    for (int i = 0; i < 2; ++i)
#pragma unroll
      for (int s = 0; s < 2; ++s) acc[i][s] = (f32x4){0.f, 0.f, 0.f, 0.f};
#pragma unroll
    for (int ks = 0; ks < 14; ++ks) {
      bf16x8 xf[2];
#pragma unroll
      for (int s = 0; s < 2; ++s)
        xf[s] = *(const bf16x8*)(smem + K2_AT + (16 * s + r) * 1024 + (((4 * ks + q) ^ r) * 16));
#pragma unroll
      for (int i = 0; i < 2; ++i)
#pragma unroll
        for (int s = 0; s < 2; ++s) acc[i][s] = mfma16(wg[i][ks], xf[s], acc[i][s]);
    }
#pragma unroll
    for (int s = 0; s < 2; ++s)
#pragma unroll
      for (int i = 0; i < 2; ++i) {
        float zz[4];
#pragma unroll
        for (int j = 0; j < 4; ++j) zz[j] = gelu_tanh(rbf(acc[i][s][j]));
        u32x2 o = {pack2(zz[0], zz[1]), pack2(zz[2], zz[3])};
        *(u32x2*)(z1 + (size_t)(((rb * 2 + s) * 56 + cb * 14 + 2 * wave + i) * 64 + lane) * 4) = o;
      }
    STAMP(1, 8);
  }
  __syncthreads();
  STAMP(1, 9);
  if (wave < 7) {
    const float2* fs = (const float2*)(smem + K2_FS);
#pragma unroll
    for (int s = 0; s < 2; ++s) {
      const f32x4 sa = *(const f32x4*)(fs + 16 * s + 4 * q), sb = *(const f32x4*)(fs + 16 * s + 4 * q + 2);
#pragma unroll
      for (int i = 0; i < 2; ++i) {
        const float n0 = rbf(lo2f(zp[i][s][0]) - sa[0]) * sa[1], n1 = rbf(hi2f(zp[i][s][0]) - sa[2]) * sa[3];
        const float n2 = rbf(lo2f(zp[i][s][1]) - sb[0]) * sb[1], n3 = rbf(hi2f(zp[i][s][1]) - sb[2]) * sb[3];
        u32x2 o = {pack2(n0, n1), pack2(n2, n3)};
        const int ftile = cb * 14 + 2 * wave + i;
        const int lq = 2 * s + (q >> 1);
        *(u32x2*)(z2T + (size_t)((ftile * 64 + rb) * 64 + 16 * lq + r) * 8 + 4 * (q & 1)) = o;
      }
    }
  }
  STAMP(1, 10);
}

constexpr int K3_WB = 0;
constexpr int K3_LDS = 3 * 16384;
constexpr int K3_D = 4;

__global__ __launch_bounds__(512) void k3_gemm2(
    const u16* __restrict__ w_sp, const u16* __restrict__ b_sp, const u16* __restrict__ z2T,
    const u16* __restrict__ z1, u16* __restrict__ uout) {
  __shared__ __attribute__((aligned(16))) char smem[K3_LDS];
  const int tid = threadIdx.x, lane = tid & 63;
  const int wave = __builtin_amdgcn_readfirstlane(tid >> 6);
  STAMP(2, 0);
  const int xcd = blockIdx.x & 7, idx = blockIdx.x >> 3;
  const int tb = (xcd & 3) * 8 + (idx & 7), fb = (xcd >> 2) * 4 + (idx >> 3);
  const int T0 = tb * 64;
  const int r = lane & 15, q = lane >> 4;
  const int kh = wave >> 2, mp = wave & 3;
  constexpr int NT = 16;

  const u16* wsrc = w_sp + (size_t)(T0 + (tid >> 4)) * S_ + (tid & 15) * 8;
  const int ft0 = fb * 7 + 2 * mp, ft1 = fb * 7 + (mp < 3 ? 2 * mp + 1 : 2 * mp);
  const u16* zsrc0 = z2T + (size_t)((ft0 * 64 + kh) * 64 + lane) * 8;
  const u16* zsrc1 = z2T + (size_t)((ft1 * 64 + kh) * 64 + lane) * 8;
  const int wdst = (tid >> 4) * 256 + (((tid & 15) ^ ((tid >> 4) & 15)) * 16);

  u32x4 wreg[K3_D][2];
  bf16x8 zreg[K3_D][2][2];
#pragma unroll
  for (int d = 0; d < K3_D; ++d) {
#pragma unroll
    for (int j = 0; j < 2; ++j) wreg[d][j] = *(const u32x4*)(wsrc + (size_t)(32 * j) * S_ + d * 128);
#pragma unroll
    for (int k2 = 0; k2 < 2; ++k2) {
      zreg[d][0][k2] = *(const bf16x8*)(zsrc0 + (d * 4 + 2 * k2) * 512);
      zreg[d][1][k2] = *(const bf16x8*)(zsrc1 + (d * 4 + 2 * k2) * 512);
    }
  }
  auto stageW = [&](int it) {
    const int d = it % K3_D;
#pragma unroll
    for (int j = 0; j < 2; ++j) *(u32x4*)(smem + K3_WB + (it % 3) * 16384 + j * 8192 + wdst) = wreg[d][j];
    if (it + K3_D < NT) {
#pragma unroll
      for (int j = 0; j < 2; ++j) wreg[d][j] = *(const u32x4*)(wsrc + (size_t)(32 * j) * S_ + (it + K3_D) * 128);
    }
  };
  auto ldfrag = [&](bf16x8 (&bf)[4], int it, int k2) {
#pragma unroll
    for (int n = 0; n < 4; ++n)
      bf[n] = *(const bf16x8*)(smem + K3_WB + (it % 3) * 16384 + (16 * n + r) * 256 + (((4 * (2 * k2 + kh) + q) ^ r) * 16));
  };
  stageW(0);
  stageW(1);
  STAMP(2, 1);
  __syncthreads();
  STAMP(2, 2);

  f32x4 acc[2][4];
#pragma unroll
  for (int i = 0; i < 2; ++i)
#pragma unroll
    for (int n = 0; n < 4; ++n) acc[i][n] = (f32x4){0.f, 0.f, 0.f, 0.f};
  u32x2 zz1[2][2];
  float bsp[2];
  bf16x8 bfa[4], bfb[4];
  ldfrag(bfa, 0, 0);
#pragma unroll
  for (int it = 0; it < NT; ++it) {
    const int d = it % K3_D;
    ldfrag(bfb, it, 1);
    if (it + 2 < NT) stageW(it + 2);
    __builtin_amdgcn_sched_barrier(0);
#pragma unroll
    for (int n = 0; n < 4; ++n) {
      acc[0][n] = mfma16(zreg[d][0][0], bfa[n], acc[0][n]);
      acc[1][n] = mfma16(zreg[d][1][0], bfa[n], acc[1][n]);
    }
    __builtin_amdgcn_sched_barrier(0);
    if (it + 1 < NT) ldfrag(bfa, it + 1, 0);
    __builtin_amdgcn_sched_barrier(0);
#pragma unroll
    for (int n = 0; n < 4; ++n) {
      acc[0][n] = mfma16(zreg[d][0][1], bfb[n], acc[0][n]);
      acc[1][n] = mfma16(zreg[d][1][1], bfb[n], acc[1][n]);
    }
    __builtin_amdgcn_sched_barrier(0);
    if (it + K3_D < NT) {
#pragma unroll
      for (int k2 = 0; k2 < 2; ++k2) {
        zreg[d][0][k2] = *(const bf16x8*)(zsrc0 + ((it + K3_D) * 4 + 2 * k2) * 512);
        zreg[d][1][k2] = *(const bf16x8*)(zsrc1 + ((it + K3_D) * 4 + 2 * k2) * 512);
      }
    }
    if (it == NT - K3_D) {
#pragma unroll
      for (int n = 0; n < 2; ++n) {
        bsp[n] = bf2f(b_sp[T0 + 16 * (2 * kh + n) + r]);
        zz1[0][n] = *(const u32x2*)(z1 + (size_t)(((tb * 4 + 2 * kh + n) * 56 + ft0) * 64 + lane) * 4);
        zz1[1][n] = *(const u32x2*)(z1 + (size_t)(((tb * 4 + 2 * kh + n) * 56 + ft1) * 64 + lane) * 4);
      }
    }
    __syncthreads();
  }
  STAMP(2, 3);
  {
    f32x4* xch = (f32x4*)smem;
#pragma unroll
    for (int i = 0; i < 2; ++i)
#pragma unroll
      for (int n = 0; n < 2; ++n) xch[((wave * 2 + i) * 2 + n) * 64 + lane] = kh ? acc[i][n] : acc[i][2 + n];
    __syncthreads();
#pragma unroll
    for (int i = 0; i < 2; ++i)
#pragma unroll
      for (int n = 0; n < 2; ++n) {
        const f32x4 o4 = xch[(((wave ^ 4) * 2 + i) * 2 + n) * 64 + lane];
        acc[i][n] = (kh ? acc[i][2 + n] : acc[i][n]) + o4;
      }
  }
  STAMP(2, 4);
#pragma unroll
  for (int n = 0; n < 2; ++n) {
    const float bb = bsp[n];
#pragma unroll
    for (int i = 0; i < 2; ++i) {
      if (i == 1 && mp == 3) continue;
      const int ftile = i ? ft1 : ft0;
      const u32x2 zz = zz1[i][n];
      float g[4];
#pragma unroll
      for (int j = 0; j < 4; ++j) g[j] = rbf(rbf(acc[i][n][j]) + bb);
      u32x2 o = {pack2(lo2f(zz[0]) * g[0], hi2f(zz[0]) * g[1]), pack2(lo2f(zz[1]) * g[2], hi2f(zz[1]) * g[3])};
      *(u32x2*)(uout + (size_t)((((tb * 4 + 2 * kh + n) * 28 + (ftile >> 1)) * 64 + 16 * (2 * (ftile & 1) + (q >> 1)) + r) * 8 + 4 * (q & 1))) = o;
    }
  }
  STAMP(2, 5);
}

constexpr int K4_LDS = 64 * 1792;

__global__ __launch_bounds__(256) void k4_gemm3(
    const u16* __restrict__ uin, const u16* __restrict__ w_outT, const u16* __restrict__ hin,
    u16* __restrict__ out) {
  __shared__ __attribute__((aligned(16))) char smem[K4_LDS];
  const int tid = threadIdx.x, lane = tid & 63, wave = tid >> 6;
  STAMP(3, 0);
  const int xcd = blockIdx.x & 7, idx = blockIdx.x >> 3;
  const int tb = xcd * 4 + (idx & 3), db = idx >> 2;
  const int T0 = tb * 64, D0 = db * 64;
  const int r = lane & 15, q = lane >> 4;

#pragma unroll
  for (int h = 0; h < 2; ++h) {
    u32x4 ul[14];
#pragma unroll
    for (int j = 0; j < 14; ++j)
      ul[j] = *(const u32x4*)(uin + (size_t)tb * 64 * FH_ + (size_t)(tid + 256 * (14 * h + j)) * 8);
#pragma unroll
    for (int j = 0; j < 14; ++j) *(u32x4*)(smem + (tid + 256 * (14 * h + j)) * 16) = ul[j];
  }
  bf16x8 wf[28];
#pragma unroll
  for (int ks = 0; ks < 28; ++ks)
    wf[ks] = *(const bf16x8*)(w_outT + (size_t)(((db * 4 + wave) * 28 + ks) * 64 + lane) * 8);
  STAMP(3, 1);
  __syncthreads();
  STAMP(3, 2);
  f32x4 acc[4];
#pragma unroll
  for (int n = 0; n < 4; ++n) acc[n] = (f32x4){0.f, 0.f, 0.f, 0.f};
#pragma unroll
  for (int ks = 0; ks < 28; ++ks) {
#pragma unroll
    for (int n = 0; n < 4; ++n) {
      const bf16x8 b = *(const bf16x8*)(smem + ((n * 28 + ks) * 64 + lane) * 16);
      acc[n] = mfma16(wf[ks], b, acc[n]);
    }
  }
#pragma unroll
  for (int n = 0; n < 4; ++n) {
    const int t = T0 + 16 * n + r, d = D0 + 16 * wave + 4 * q;
    const u32x2 hh = *(const u32x2*)(hin + (size_t)t * D_ + d);
    u32x2 o = {pack2(lo2f(hh[0]) + rbf(acc[n][0]), hi2f(hh[0]) + rbf(acc[n][1])),
               pack2(lo2f(hh[1]) + rbf(acc[n][2]), hi2f(hh[1]) + rbf(acc[n][3]))};
    *(u32x2*)(out + (size_t)t * D_ + d) = o;
  }
  STAMP(3, 3);
}

extern "C" void kernel_launch(void* const* d_in, const int* in_sizes, int n_in, void* d_out,
                              int out_size, void* d_ws, size_t ws_size, hipStream_t stream) {
  const u16* xq = (const u16*)d_in[0];
  const u16* kin = (const u16*)d_in[1];
  const u16* vin = (const u16*)d_in[2];
  const u16* shk = (const u16*)d_in[3];
  const u16* ssmk = (const u16*)d_in[4];
  const u16* ssmD = (const u16*)d_in[5];
  const u16* w_in = (const u16*)d_in[7];
  const u16* w_sp = (const u16*)d_in[8];
  const u16* b_sp = (const u16*)d_in[9];
  const u16* w_out = (const u16*)d_in[10];
  char* ws = (char*)d_ws;
  u16* yT = (u16*)(ws + WS_YT);
  u16* h = (u16*)(ws + WS_H);
  u16* w_inT = (u16*)(ws + WS_WINT);
  u16* w_outT = (u16*)(ws + WS_WOUTT);
  float2* ln1 = (float2*)(ws + WS_LN1);
  u16* z1 = (u16*)(ws + WS_Z1);
  u16* z2T = (u16*)(ws + WS_Z2T);
  unsigned long long* pst = (unsigned long long*)(ws + WS_PST);
  unsigned* cnt = (unsigned*)(ws + WS_CNT);
  u16* u = (u16*)(ws + WS_U);

  k1_conv<<<256, 512, 0, stream>>>(kin, vin, shk, ssmk, ssmD, yT, xq, w_in, w_out, w_inT, w_outT, ln1, (unsigned*)pst);
  k2_gemm1<<<256, 512, 0, stream>>>(xq, yT, ln1, w_inT, h, z1, z2T, pst, cnt);
  k3_gemm2<<<256, 512, 0, stream>>>(w_sp, b_sp, z2T, z1, u);
  k4_gemm3<<<224, 256, 0, stream>>>(u, w_outT, h, (u16*)d_out);
}
```

```cpp
#include <hip/hip_runtime.h>
#include <hip/hip_bf16.h>
#include <stdint.h>

typedef __attribute__((ext_vector_type(8))) short bf16x8;
typedef __attribute__((ext_vector_type(4))) short s16x4;
typedef __attribute__((ext_vector_type(4))) float f32x4;
typedef __attribute__((ext_vector_type(2))) float f32x2;
typedef __attribute__((ext_vector_type(4))) unsigned int u32x4;
typedef __attribute__((ext_vector_type(2))) unsigned int u32x2;
typedef __attribute__((ext_vector_type(2))) __bf16 bf16x2_t;
typedef unsigned short u16;

#define DEVI __device__ __forceinline__
#define STAMP(kid, i)

constexpr int S_ = 2048, D_ = 448, F_ = 1792, FH_ = 896;

DEVI float lo2f(unsigned v) { return __uint_as_float(v << 16); }
DEVI float hi2f(unsigned v) { return __uint_as_float(v & 0xffff0000u); }
DEVI float bf2f(u16 v) { return __uint_as_float(((unsigned)v) << 16); }
DEVI unsigned pack2(float a, float b) {
  f32x2 f = {a, b};
  bf16x2_t h = __builtin_convertvector(f, bf16x2_t);
  return __builtin_bit_cast(unsigned, h);
}
DEVI float rbf(float x) { return lo2f(pack2(x, x)); }
DEVI f32x4 mfma16(bf16x8 a, bf16x8 b, f32x4 c) {
  return __builtin_amdgcn_mfma_f32_16x16x32_bf16(a, b, c, 0, 0, 0);
}
template <int CTRL> DEVI float dpp_f(float x) {
  return __builtin_bit_cast(float, __builtin_amdgcn_update_dpp(0, __builtin_bit_cast(int, x), CTRL, 0xf, 0xf, false));
}
DEVI float row16_sum(float x) {
  x += dpp_f<0x121>(x);
  x += dpp_f<0x122>(x);
  x += dpp_f<0x124>(x);
  x += dpp_f<0x128>(x);
  return x;
}
DEVI float gelu_tanh(float x) {
  const float u = x * (0.7978845608028654f + 0.035677408136300125f * x * x);
  const float e = __builtin_amdgcn_exp2f(-2.885390081777927f * u);
  return x * __builtin_amdgcn_rcpf(1.0f + e);
}

constexpr size_t WS_YT = 0;
constexpr size_t WS_H = WS_YT + (size_t)D_ * S_ * 2;
constexpr size_t WS_WINT = WS_H + (size_t)S_ * D_ * 2;
constexpr size_t WS_WOUTT = WS_WINT + (size_t)F_ * D_ * 2;
constexpr size_t WS_LN1 = WS_WOUTT + (size_t)D_ * FH_ * 2;
constexpr size_t WS_Z1 = WS_LN1 + (size_t)S_ * 8;
constexpr size_t WS_Z2T = WS_Z1 + (size_t)S_ * FH_ * 2;
constexpr size_t WS_PST = WS_Z2T + (size_t)S_ * FH_ * 2;
constexpr size_t WS_U = WS_PST + (size_t)9 * S_ * 8;
constexpr size_t WS_KVR = WS_U + (size_t)S_ * FH_ * 2;

constexpr int K1_XREV = 0;
constexpr int K1_XSTRIDE = 2304 * 2;
constexpr int K1_COP = 9216;
constexpr int K1_CSTRIDE = 4128;
constexpr int K1_RED = K1_COP + 2 * 8 * K1_CSTRIDE;
constexpr int K1_LDS = K1_RED + 8 * 8192;

DEVI float wave_sum(float x) {
  x = row16_sum(x);
  const int xi = __builtin_bit_cast(int, x);
  return __builtin_bit_cast(float, __builtin_amdgcn_readlane(xi, 0)) + __builtin_bit_cast(float, __builtin_amdgcn_readlane(xi, 16)) +
         __builtin_bit_cast(float, __builtin_amdgcn_readlane(xi, 32)) + __builtin_bit_cast(float, __builtin_amdgcn_readlane(xi, 48));
}

__global__ __launch_bounds__(512) void k0_prep(
    const u16* __restrict__ kin, const u16* __restrict__ vin, const u16* __restrict__ shk,
    u16* __restrict__ kvr,
    const u16* __restrict__ xin, const u16* __restrict__ w_in, const u16* __restrict__ w_out,
    u16* __restrict__ w_inT, u16* __restrict__ w_outT, float2* __restrict__ ln1,
    unsigned long long* __restrict__ gran) {
  __shared__ __attribute__((aligned(16))) char smem[8 * 4608 + 64 * 144];
  const int tid = threadIdx.x, lane = tid & 63;
  const int wave = __builtin_amdgcn_readfirstlane(tid >> 6);
  const int blk = blockIdx.x;
  STAMP(4, 0);
  const int gw = blk * 8 + wave;
  const int r = lane & 15, q = lane >> 4;

  const bool has_tile = blk < 224;
  const int S0 = (blk / 7) * 64, D0 = (blk % 7) * 64;
  const int row = tid >> 3, c8 = (tid & 7) * 8;
  u32x4 kq[4], vq = {0u, 0u, 0u, 0u}, sk[4];
  if (has_tile) {
    const int s = S0 + row;
#pragma unroll
    for (int j = 0; j < 4; ++j) {
      kq[j] = (u32x4){0u, 0u, 0u, 0u};
      if (s - 3 + j >= 0) kq[j] = *(const u32x4*)(kin + (size_t)(s - 3 + j) * D_ + D0 + c8);
    }
    vq = *(const u32x4*)(vin + (size_t)s * D_ + D0 + c8);
#pragma unroll
    for (int j = 0; j < 4; ++j) sk[j] = *(const u32x4*)(shk + (D0 + c8) * 4 + j * 8);
  }
  u32x4 xr = {0u, 0u, 0u, 0u};
  if (lane < 56) xr = *(const u32x4*)(xin + (size_t)gw * D_ + lane * 8);
  const bool has_slab = gw < 588;
  u32x4 sl[4];
  if (has_slab) {
    const u16* src; int ld;
    if (gw < 392) { src = w_in + (size_t)(gw / 28) * 32 * F_ + (gw % 28) * 64; ld = F_; }
    else { const int i2 = gw - 392; src = w_out + (size_t)(i2 / 7) * 32 * D_ + (i2 % 7) * 64; ld = D_; }
#pragma unroll
    for (int i = 0; i < 4; ++i)
      sl[i] = *(const u32x4*)(src + (size_t)(8 * i + (lane >> 3)) * ld + (lane & 7) * 8);
  }
  if (blk < 33) gran[blk * 512 + tid] = 0ull;

  STAMP(4, 1);
  u16* tl = (u16*)(smem + 8 * 4608);
  if (has_tile) {
#pragma unroll
    for (int e = 0; e < 8; ++e) {
      const unsigned s01 = sk[e >> 1][(e & 1) * 2], s23 = sk[e >> 1][(e & 1) * 2 + 1];
      float kf[4];
#pragma unroll
      for (int j = 0; j < 4; ++j) kf[j] = (e & 1) ? hi2f(kq[j][e >> 1]) : lo2f(kq[j][e >> 1]);
      float a = rbf(kf[0] * hi2f(s23));
      a = rbf(a + rbf(kf[1] * lo2f(s23)));
      a = rbf(a + rbf(kf[2] * hi2f(s01)));
      a = rbf(a + rbf(kf[3] * lo2f(s01)));
      const float vf = (e & 1) ? hi2f(vq[e >> 1]) : lo2f(vq[e >> 1]);
      tl[(c8 + e) * 72 + (63 - row)] = (u16)(pack2(a * vf, 0.f) & 0xffffu);
    }
  }
  STAMP(4, 2);
  {
    float v[8];
#pragma unroll
    for (int j = 0; j < 4; ++j) { v[2 * j] = lo2f(xr[j]); v[2 * j + 1] = hi2f(xr[j]); }
    float sm = 0.f;
#pragma unroll
    for (int j = 0; j < 8; ++j) sm += v[j];
    const float mean = wave_sum(sm) * (1.0f / D_);
    float m2 = 0.f;
    if (lane < 56) {
#pragma unroll
      for (int j = 0; j < 8; ++j) { float d = v[j] - mean; m2 += d * d; }
    }
    m2 = wave_sum(m2);
    if (lane == 0) {
      const float var = rbf(m2 * (1.0f / D_));
      const float sd = rbf(sqrtf(rbf(var + 1e-5f)));
      ln1[gw] = make_float2(rbf(mean), 1.0f / sd);
    }
  }
  STAMP(4, 3);
  if (has_slab) {
    typedef __attribute__((address_space(3))) s16x4* lp;
    char* ts = smem + wave * 4608;
    u16* dst; int bstride;
    if (gw < 392) { dst = w_inT + (size_t)((gw % 28) * 4 * 14 + (gw / 28)) * 512; bstride = 14 * 512; }
    else { const int i2 = gw - 392; dst = w_outT + (size_t)((i2 % 7) * 4 * 28 + (i2 / 7)) * 512; bstride = 28 * 512; }
#pragma unroll
    for (int i = 0; i < 4; ++i) *(u32x4*)(ts + (8 * i + (lane >> 3)) * 144 + (lane & 7) * 16) = sl[i];
#pragma unroll
    for (int n = 0; n < 4; ++n) {
      const char* a0p = ts + (8 * q + (r >> 2)) * 144 + (16 * n + 4 * (r & 3)) * 2;
      s16x4 v0 = __builtin_amdgcn_ds_read_tr16_b64_v4i16((lp)(a0p));
      s16x4 v1 = __builtin_amdgcn_ds_read_tr16_b64_v4i16((lp)(a0p + 4 * 144));
      bf16x8 o = {v0[0], v0[1], v0[2], v0[3], v1[0], v1[1], v1[2], v1[3]};
      *(bf16x8*)(dst + (size_t)n * bstride + lane * 8) = o;
    }
  }
  STAMP(4, 4);
  if (has_tile) {
    __syncthreads();
    const u32x4 o = *(const u32x4*)(tl + row * 72 + c8);
    *(u32x4*)(kvr + (size_t)(D0 + row) * S_ + (1984 - S0) + c8) = o;
  }
  STAMP(4, 5);
}

__global__ __launch_bounds__(512) void k1_conv(
    const u16* __restrict__ kvr, const u16* __restrict__ ssmk, const u16* __restrict__ ssmD,
    u16* __restrict__ yT, const u16* __restrict__ w_sp) {
  __shared__ __attribute__((aligned(16))) char smem[K1_LDS];
  const int tid = threadIdx.x, lane = tid & 63, wave = tid >> 6;
  STAMP(0, 0);
  const int xcd = blockIdx.x & 7, idx = blockIdx.x >> 3;
  const int d0 = (xcd * 28 + idx) * 2;
  const int tch = tid >> 8, mm = tid & 255;
  const u16* krow = ssmk + (size_t)(d0 + tch) * S_;
  const u32x4 xr = *(const u32x4*)(kvr + (size_t)(d0 + tch) * S_ + mm * 8);
  u32x4 wa = *(const u32x4*)(krow + mm * 8);
  u32x4 wb = {0u, 0u, 0u, 0u};
  if (mm < 255) wb = *(const u32x4*)(krow + mm * 8 + 8);
  const unsigned dDw = *(const unsigned*)(ssmD + d0);
  u32x4 pf[5];
  {
    const int base = (xcd * 28 + idx) * 2560 + tid;
#pragma unroll
    for (int j = 0; j < 5; ++j) {
      const int c = base + j * 512;
      pf[j] = ((const u32x4*)w_sp)[c < S_ * S_ / 8 ? c : 0];
    }
  }
  *(u32x4*)(smem + K1_XREV + tch * K1_XSTRIDE + mm * 16) = xr;

  if (tid < 64) {
    u32x4 z = {0u, 0u, 0u, 0u};
    *(u32x4*)(smem + K1_XREV + (tid >> 5) * K1_XSTRIDE + (2048 + (tid & 31) * 8) * 2) = z;
  }
  {
    unsigned w[8] = {wa[0], wa[1], wa[2], wa[3], wb[0], wb[1], wb[2], wb[3]};
    unsigned al[7];
#pragma unroll
    for (int i = 0; i < 7; ++i) al[i] = (w[i] >> 16) | (w[i + 1] << 16);
    char* base = smem + K1_COP + tch * 8 * K1_CSTRIDE + (mm + 2) * 16;
#pragma unroll
    for (int c = 0; c < 8; ++c) {
      u32x4 o;
      if ((c & 1) == 0) { o[0] = w[c / 2]; o[1] = w[c / 2 + 1]; o[2] = w[c / 2 + 2]; o[3] = w[c / 2 + 3]; }
      else { o[0] = al[c / 2]; o[1] = al[c / 2 + 1]; o[2] = al[c / 2 + 2]; o[3] = al[c / 2 + 3]; }
      *(u32x4*)(base + c * K1_CSTRIDE) = o;
    }
    if (mm < 2) {
      unsigned w2[8] = {0u, 0u, 0u, 0u, 0u, 0u, 0u, 0u};
      if (mm == 0) { w2[4] = wa[0]; w2[5] = wa[1]; w2[6] = wa[2]; w2[7] = wa[3]; }
      unsigned a2[7];
#pragma unroll
      for (int i = 0; i < 7; ++i) a2[i] = (w2[i] >> 16) | (w2[i + 1] << 16);
      char* b2 = smem + K1_COP + tch * 8 * K1_CSTRIDE + (1 - mm) * 16;
#pragma unroll
      for (int c = 0; c < 8; ++c) {
        u32x4 o;
        if ((c & 1) == 0) { o[0] = w2[c / 2]; o[1] = w2[c / 2 + 1]; o[2] = w2[c / 2 + 2]; o[3] = w2[c / 2 + 3]; }
        else { o[0] = a2[c / 2]; o[1] = a2[c / 2 + 1]; o[2] = a2[c / 2 + 2]; o[3] = a2[c / 2 + 3]; }
        *(u32x4*)(b2 + c * K1_CSTRIDE) = o;
      }
    }
  }
  STAMP(0, 1);
  __syncthreads();
  STAMP(0, 2);

  {
    const int ch = wave & 1, r = lane & 15, q = lane >> 4;
    f32x4 acc[8];
#pragma unroll
    for (int t = 0; t < 8; ++t) acc[t] = (f32x4){0.f, 0.f, 0.f, 0.f};
    const char* abase = smem + K1_COP + ch * 8 * K1_CSTRIDE + ((r + 1) & 7) * K1_CSTRIDE +
                        (2 * (q >> 1) - (q & 1) + ((r + 9) >> 3)) * 16;
    const char* bbase = smem + K1_XREV + ch * K1_XSTRIDE + (2040 - 16 * r + 16 * (q >> 1) - 8 * (q & 1)) * 2;
#pragma unroll
    for (int cc = 0; cc < 2; ++cc) {
      const int rho = 2 * (wave >> 1) + cc;
      bf16x8 A[8], B[8];
#pragma unroll
      for (int i = 0; i < 8; ++i) A[i] = *(const bf16x8*)(abase + (rho + 8 * i) * 64);
#pragma unroll
      for (int i = 0; i < 8; ++i) B[i] = *(const bf16x8*)(bbase - (8 * i - rho) * 64);
#pragma unroll
      for (int t = 0; t < 8; ++t)
#pragma unroll
        for (int i = 0; i <= t; ++i) acc[t] = mfma16(A[i], B[t - i], acc[t]);
    }
    float* red = (float*)(smem + K1_RED + wave * 8192);
#pragma unroll
    for (int t = 0; t < 8; ++t) *(f32x4*)(red + 256 * t + 16 * r + 4 * q) = acc[t];
  }
  STAMP(0, 3);
  __syncthreads();
  STAMP(0, 4);
  {
    const int ch = tid >> 8, e0 = (tid & 255) * 8;
    f32x4 sa = {0.f, 0.f, 0.f, 0.f}, sb = {0.f, 0.f, 0.f, 0.f};
#pragma unroll
    for (int w = 0; w < 4; ++w) {
      const float* red = (const float*)(smem + K1_RED + (2 * w + ch) * 8192);
      sa += *(const f32x4*)(red + e0);
      sb += *(const f32x4*)(red + e0 + 4);
    }
    const u32x4 kr = *(const u32x4*)(smem + K1_XREV + ch * K1_XSTRIDE + (2040 - e0) * 2);
    const float Dv = ch ? hi2f(dDw) : lo2f(dDw);
    float kvf[8];
    kvf[7] = lo2f(kr[0]); kvf[6] = hi2f(kr[0]); kvf[5] = lo2f(kr[1]); kvf[4] = hi2f(kr[1]);
    kvf[3] = lo2f(kr[2]); kvf[2] = hi2f(kr[2]); kvf[1] = lo2f(kr[3]); kvf[0] = hi2f(kr[3]);
    float y[8];
#pragma unroll
    for (int j = 0; j < 4; ++j) {
      y[j] = rbf(sa[j]) + rbf(kvf[j] * Dv);
      y[j + 4] = rbf(sb[j]) + rbf(kvf[j + 4] * Dv);
    }
    u32x4 o = {pack2(y[0], y[1]), pack2(y[2], y[3]), pack2(y[4], y[5]), pack2(y[6], y[7])};
    *(u32x4*)(yT + (size_t)(d0 + ch) * S_ + e0) = o;
  }
#pragma unroll
  for (int j = 0; j < 5; ++j) asm volatile("" ::"v"(pf[j]));
  STAMP(0, 5);
}

constexpr int K2_YT = 0;
constexpr int K2_AT = 448 * 80;
constexpr int K2_ST = K2_AT + 32 * 1024;
constexpr int K2_FS = K2_ST + 8 * 32 * 8;
constexpr int K2_LDS = K2_FS + 32 * 8;

DEVI f32x2 unp(unsigned v) { return (f32x2){lo2f(v), hi2f(v)}; }
DEVI unsigned pk(f32x2 v) { return pack2(v[0], v[1]); }

__global__ __launch_bounds__(512) void k2_gemm1(
    const u16* __restrict__ xin, const u16* __restrict__ yT, const float2* __restrict__ ln1,
    const u16* __restrict__ w_inT, u16* __restrict__ hout, u16* __restrict__ z1,
    u16* __restrict__ z2T, unsigned long long* pstat) {
  __shared__ __attribute__((aligned(16))) char smem[K2_LDS];
  const int tid = threadIdx.x, lane = tid & 63;
  const int wave = __builtin_amdgcn_readfirstlane(tid >> 6);
  STAMP(1, 0);
  const int xcd = blockIdx.x & 7, idx = blockIdx.x >> 3;
  const int rb = xcd * 8 + (idx & 7), cb = idx >> 3;
  const int R0 = rb * 32;
  const int r = lane & 15, q = lane >> 4;

  u32x4 yl[4], xg[4];
#pragma unroll
  for (int j = 0; j < 4; ++j) {
    const int id = tid + 512 * j;
    if (j < 3 || tid < 256) {
      yl[j] = *(const u32x4*)(yT + (size_t)(id >> 2) * S_ + R0 + (id & 3) * 8);
      xg[j] = *(const u32x4*)(xin + (size_t)R0 * D_ + id * 8);
    }
  }
  const int prow = 16 * (wave & 1) + r;
  const int dq0 = (wave >> 1) * 28 + q;
  const float2 l1 = ln1[R0 + prow];
  const int wv = wave < 7 ? wave : 6;
  const u16* wz2 = w_inT + (size_t)((56 + cb * 14 + 2 * wv) * 14 * 64 + lane) * 8;
  const u16* wz1 = w_inT + (size_t)((cb * 14 + 2 * wv) * 14 * 64 + lane) * 8;
  bf16x8 wf[2][14];
#pragma unroll
  for (int ks = 0; ks < 7; ++ks)
#pragma unroll
    for (int i = 0; i < 2; ++i) wf[i][ks] = *(const bf16x8*)(wz2 + (i * 14 + ks) * 512);
#pragma unroll
  for (int j = 0; j < 4; ++j) {
    const int id = tid + 512 * j;
    if (j < 3 || tid < 256) {
      *(u32x4*)(smem + K2_YT + (id >> 2) * 80 + (id & 3) * 16) = yl[j];
      const int row = id / 56, c = id % 56;
      *(u32x4*)(smem + K2_AT + row * 1024 + ((c ^ (row & 15)) * 16)) = xg[j];
    }
  }
  STAMP(1, 1);
  __syncthreads();
  STAMP(1, 2);
  f32x2 hv[7][2];
  f32x2 sum2 = {0.f, 0.f};
  {
    typedef __attribute__((address_space(3))) s16x4* lp;
    const int qq = r >> 2, pp = r & 3;
    const f32x2 m1 = {l1.x, l1.x}, r1 = {l1.y, l1.y};
#pragma unroll
    for (int it = 0; it < 7; ++it) {
      const int dq = dq0 + 4 * it;
      const char* a0 = smem + K2_YT + (4 * dq + qq) * 80 + (16 * (wave & 1) + 4 * pp) * 2;
      const u32x2 y0 = __builtin_bit_cast(u32x2, __builtin_amdgcn_ds_read_tr16_b64_v4i16((lp)(a0)));
      const u32x2 xv = *(const u32x2*)(smem + K2_AT + prow * 1024 + (((dq >> 1) ^ r) * 16) + (dq & 1) * 8);
#pragma unroll
      for (int j = 0; j < 2; ++j) {
        const f32x2 x2 = unp(xv[j]);
        const unsigned dd = pk(x2 - m1);
        const unsigned qn = pk(unp(dd) * r1);
        const unsigned aa = pk(unp(qn) * unp(y0[j]));
        hv[it][j] = unp(pk(x2 + unp(aa)));
        sum2 += hv[it][j];
      }
#pragma unroll
      for (int i = 0; i < 2; ++i) wf[i][7 + it] = *(const bf16x8*)(wz2 + (i * 14 + 7 + it) * 512);
      __builtin_amdgcn_sched_barrier(0);
    }
  }
  float sum = sum2[0] + sum2[1];
  sum += __shfl_xor(sum, 16);
  sum += __shfl_xor(sum, 32);
  const float mh = sum * (1.0f / 112.0f);
  f32x2 q2 = {0.f, 0.f};
  {
    const f32x2 mh2 = {mh, mh};
#pragma unroll
    for (int it = 0; it < 7; ++it)
#pragma unroll
      for (int j = 0; j < 2; ++j) {
        const f32x2 d = hv[it][j] - mh2;
        q2 += d * d;
      }
  }
  float m2 = q2[0] + q2[1];
  m2 += __shfl_xor(m2, 16);
  m2 += __shfl_xor(m2, 32);
  float2* st = (float2*)(smem + K2_ST);
  if (q == 0) st[wave * 16 + r] = make_float2(mh, m2);
  STAMP(1, 3);
  __syncthreads();
  STAMP(1, 4);
  {
    float2 pq[4];
    float ms = 0.f;
#pragma unroll
    for (int w = 0; w < 4; ++w) { pq[w] = st[((wave & 1) + 2 * w) * 16 + r]; ms += pq[w].x; }
    const float mean = 0.25f * ms;
    float M2 = 0.f;
#pragma unroll
    for (int w = 0; w < 4; ++w) { const float dl = pq[w].x - mean; M2 += pq[w].y + 112.0f * dl * dl; }
    const float var = rbf(M2 * (1.0f / D_));
    const float mb = rbf(mean);
    const float rinv = 1.0f / rbf(sqrtf(rbf(var + 1e-5f)));
    const f32x2 mb2 = {mb, mb}, ri2 = {rinv, rinv};
#pragma unroll
    for (int it = 0; it < 7; ++it) {
      u32x2 o2;
#pragma unroll
      for (int j = 0; j < 2; ++j) o2[j] = pk(unp(pk(hv[it][j] - mb2)) * ri2);
      const int dq = dq0 + 4 * it;
      *(u32x2*)(smem + K2_AT + prow * 1024 + (((dq >> 1) ^ r) * 16) + (dq & 1) * 8) = o2;
      if (cb == 0) {
        u32x2 h2 = {pk(hv[it][0]), pk(hv[it][1])};
        *(u32x2*)(hout + (size_t)(R0 + prow) * D_ + dq * 4) = h2;
      }
    }
  }
  STAMP(1, 5);
  __syncthreads();
  STAMP(1, 6);

  u32x2 zp[2][2];
  bf16x8 wg[2][14];
  if (wave < 7) {
    f32x4 acc[2][2];
#pragma unroll
    for (int i = 0; i < 2; ++i)
#pragma unroll
      for (int s = 0; s < 2; ++s) acc[i][s] = (f32x4){0.f, 0.f, 0.f, 0.f};
#pragma unroll
    for (int ks = 0; ks < 14; ++ks) {
      bf16x8 xf[2];
#pragma unroll
      for (int s = 0; s < 2; ++s)
        xf[s] = *(const bf16x8*)(smem + K2_AT + (16 * s + r) * 1024 + (((4 * ks + q) ^ r) * 16));
#pragma unroll
      for (int i = 0; i < 2; ++i)
#pragma unroll
        for (int s = 0; s < 2; ++s) acc[i][s] = mfma16(xf[s], wf[i][ks], acc[i][s]);
#pragma unroll
      for (int i = 0; i < 2; ++i) wg[i][ks] = *(const bf16x8*)(wz1 + (i * 14 + ks) * 512);
    }
#pragma unroll
    for (int s = 0; s < 2; ++s) {
#pragma unroll
      for (int i = 0; i < 2; ++i) {
        float zz[4];
#pragma unroll
        for (int j = 0; j < 4; ++j) zz[j] = gelu_tanh(rbf(acc[i][s][j]));
        zp[i][s] = (u32x2){pack2(zz[0], zz[1]), pack2(zz[2], zz[3])};
      }
      float mw[4], qw[4];
#pragma unroll
      for (int j = 0; j < 4; ++j) {
        const float v0 = (j & 1) ? hi2f(zp[0][s][j >> 1]) : lo2f(zp[0][s][j >> 1]);
        const float v1 = (j & 1) ? hi2f(zp[1][s][j >> 1]) : lo2f(zp[1][s][j >> 1]);
        mw[j] = row16_sum(v0 + v1) * (1.0f / 32.0f);
        const float a0 = v0 - mw[j], a1 = v1 - mw[j];
        qw[j] = row16_sum(a0 * a0 + a1 * a1);
      }
      if (r == 0) {
#pragma unroll
        for (int j = 0; j < 4; ++j) st[wave * 32 + 16 * s + 4 * q + j] = make_float2(mw[j], qw[j]);
      }
    }
  }
  __syncthreads();
  if (wave == 7) {
    float2* fs = (float2*)(smem + K2_FS);
    const int row = lane >> 1;
    float msum = 0.f;
    float2 pw[7];
#pragma unroll
    for (int w = 0; w < 7; ++w) { pw[w] = st[w * 32 + row]; msum += pw[w].x; }
    const float bmean = msum * (1.0f / 7.0f);
    float bM2 = 0.f;
#pragma unroll
    for (int w = 0; w < 7; ++w) { const float dl = pw[w].x - bmean; bM2 += pw[w].y + 32.0f * dl * dl; }
    const float mine = (lane & 1) ? bM2 : bmean;
    unsigned long long* g = pstat + (size_t)rb * 256 + lane;
    __hip_atomic_store(g + cb * 64, (1ull << 32) | __float_as_uint(mine), __ATOMIC_RELAXED, __HIP_MEMORY_SCOPE_AGENT);
    float pv[4];
    for (int spin = 0; spin < (1 << 22); ++spin) {
      bool ok = true;
#pragma unroll
      for (int c = 0; c < 4; ++c) {
        const unsigned long long v = __hip_atomic_load(g + c * 64, __ATOMIC_RELAXED, __HIP_MEMORY_SCOPE_AGENT);
        pv[c] = __uint_as_float((unsigned)v);
        ok &= (v >> 32) == 1ull;
      }
      if (__all(ok)) break;
      __builtin_amdgcn_s_sleep(1);
    }
    float pm[4], pq[4];
#pragma unroll
    for (int c = 0; c < 4; ++c) {
      const float o = __shfl_xor(pv[c], 1);
      pm[c] = (lane & 1) ? o : pv[c];
      pq[c] = (lane & 1) ? pv[c] : o;
    }
    const float mean = (pm[0] + pm[1] + pm[2] + pm[3]) * 0.25f;
    float M2 = 0.f;
#pragma unroll
    for (int c = 0; c < 4; ++c) { const float dl = pm[c] - mean; M2 += pq[c] + 224.0f * dl * dl; }
    const float var = rbf(M2 * (1.0f / FH_));
    if ((lane & 1) == 0) fs[row] = make_float2(rbf(mean), 1.0f / rbf(sqrtf(rbf(var + 1e-5f))));
  } else {
    STAMP(1, 7);
    f32x4 acc[2][2];
#pragma unroll
    for (int i = 0; i < 2; ++i)
#pragma unroll
      for (int s = 0; s < 2; ++s) acc[i][s] = (f32x4){0.f, 0.f, 0.f, 0.f};
#pragma unroll
    for (int ks = 0; ks < 14; ++ks) {
      bf16x8 xf[2];
#pragma unroll
      for (int s = 0; s < 2; ++s)
        xf[s] = *(const bf16x8*)(smem + K2_AT + (16 * s + r) * 1024 + (((4 * ks + q) ^ r) * 16));
#pragma unroll
      for (int i = 0; i < 2; ++i)
#pragma unroll
        for (int s = 0; s < 2; ++s) acc[i][s] = mfma16(wg[i][ks], xf[s], acc[i][s]);
    }
#pragma unroll
    for (int s = 0; s < 2; ++s)
#pragma unroll
      for (int i = 0; i < 2; ++i) {
        float zz[4];
#pragma unroll
        for (int j = 0; j < 4; ++j) zz[j] = gelu_tanh(rbf(acc[i][s][j]));
        u32x2 o = {pack2(zz[0], zz[1]), pack2(zz[2], zz[3])};
        *(u32x2*)(z1 + (size_t)(((rb * 2 + s) * 56 + cb * 14 + 2 * wave + i) * 64 + lane) * 4) = o;
      }
    STAMP(1, 8);
  }
  __syncthreads();
  STAMP(1, 9);
  if (wave < 7) {
    const float2* fs = (const float2*)(smem + K2_FS);
#pragma unroll
    for (int s = 0; s < 2; ++s) {
      const f32x4 sa = *(const f32x4*)(fs + 16 * s + 4 * q), sb = *(const f32x4*)(fs + 16 * s + 4 * q + 2);
#pragma unroll
      for (int i = 0; i < 2; ++i) {
        const float n0 = rbf(lo2f(zp[i][s][0]) - sa[0]) * sa[1], n1 = rbf(hi2f(zp[i][s][0]) - sa[2]) * sa[3];
        const float n2 = rbf(lo2f(zp[i][s][1]) - sb[0]) * sb[1], n3 = rbf(hi2f(zp[i][s][1]) - sb[2]) * sb[3];
        u32x2 o = {pack2(n0, n1), pack2(n2, n3)};
        const int ftile = cb * 14 + 2 * wave + i;
        const int lq = 2 * s + (q >> 1);
        *(u32x2*)(z2T + (size_t)((ftile * 64 + rb) * 64 + 16 * lq + r) * 8 + 4 * (q & 1)) = o;
      }
    }
  }
  STAMP(1, 10);
}

constexpr int K3_WB = 0;
constexpr int K3_LDS = 64 * 1792;
constexpr int K3_D = 4;

__global__ __launch_bounds__(512) void k3_gemm2(
    const u16* __restrict__ w_sp, const u16* __restrict__ b_sp, const u16* __restrict__ z2T,
    const u16* __restrict__ z1, u16* uout, const u16* __restrict__ w_outT,
    const u16* __restrict__ hin, u16* __restrict__ out, unsigned* cnt) {
  __shared__ __attribute__((aligned(16))) char smem[K3_LDS];
  const int tid = threadIdx.x, lane = tid & 63;
  const int wave = __builtin_amdgcn_readfirstlane(tid >> 6);
  STAMP(2, 0);
  const int xcd = blockIdx.x & 7, idx = blockIdx.x >> 3;
  const int tb = (xcd & 3) * 8 + (idx & 7), fb = (xcd >> 2) * 4 + (idx >> 3);
  const int T0 = tb * 64;
  const int r = lane & 15, q = lane >> 4;
  const int kh = wave >> 2, mp = wave & 3;
  constexpr int NT = 16;

  const u16* wsrc = w_sp + (size_t)(T0 + (tid >> 4)) * S_ + (tid & 15) * 8;
  const int ft0 = fb * 7 + 2 * mp, ft1 = fb * 7 + (mp < 3 ? 2 * mp + 1 : 2 * mp);
  const u16* zsrc0 = z2T + (size_t)((ft0 * 64 + kh) * 64 + lane) * 8;
  const u16* zsrc1 = z2T + (size_t)((ft1 * 64 + kh) * 64 + lane) * 8;
  const int wdst = (tid >> 4) * 256 + (((tid & 15) ^ ((tid >> 4) & 15)) * 16);

  u32x4 wreg[K3_D][2];
  bf16x8 zreg[K3_D][2][2];
#pragma unroll
  for (int d = 0; d < K3_D; ++d) {
#pragma unroll
    for (int j = 0; j < 2; ++j) wreg[d][j] = *(const u32x4*)(wsrc + (size_t)(32 * j) * S_ + d * 128);
#pragma unroll
    for (int k2 = 0; k2 < 2; ++k2) {
      zreg[d][0][k2] = *(const bf16x8*)(zsrc0 + (d * 4 + 2 * k2) * 512);
      zreg[d][1][k2] = *(const bf16x8*)(zsrc1 + (d * 4 + 2 * k2) * 512);
    }
  }
  auto stageW = [&](int it) {
    const int d = it % K3_D;
#pragma unroll
    for (int j = 0; j < 2; ++j) *(u32x4*)(smem + K3_WB + (it % 3) * 16384 + j * 8192 + wdst) = wreg[d][j];
    if (it + K3_D < NT) {
#pragma unroll
      for (int j = 0; j < 2; ++j) wreg[d][j] = *(const u32x4*)(wsrc + (size_t)(32 * j) * S_ + (it + K3_D) * 128);
    }
  };
  auto ldfrag = [&](bf16x8 (&bf)[4], int it, int k2) {
#pragma unroll
    for (int n = 0; n < 4; ++n)
      bf[n] = *(const bf16x8*)(smem + K3_WB + (it % 3) * 16384 + (16 * n + r) * 256 + (((4 * (2 * k2 + kh) + q) ^ r) * 16));
  };
  stageW(0);
  stageW(1);
  STAMP(2, 1);
  __syncthreads();
  STAMP(2, 2);

  f32x4 acc[2][4];
#pragma unroll
  for (int i = 0; i < 2; ++i)
#pragma unroll
    for (int n = 0; n < 4; ++n) acc[i][n] = (f32x4){0.f, 0.f, 0.f, 0.f};
  u32x2 zz1[2][2];
  float bsp[2];
  bf16x8 bfa[4], bfb[4];
  ldfrag(bfa, 0, 0);
#pragma unroll
  for (int it = 0; it < NT; ++it) {
    const int d = it % K3_D;
    ldfrag(bfb, it, 1);
    if (it + 2 < NT) stageW(it + 2);
    __builtin_amdgcn_sched_barrier(0);
#pragma unroll
    for (int n = 0; n < 4; ++n) {
      acc[0][n] = mfma16(zreg[d][0][0], bfa[n], acc[0][n]);
      acc[1][n] = mfma16(zreg[d][1][0], bfa[n], acc[1][n]);
    }
    __builtin_amdgcn_sched_barrier(0);
    if (it + 1 < NT) ldfrag(bfa, it + 1, 0);
    __builtin_amdgcn_sched_barrier(0);
#pragma unroll
    for (int n = 0; n < 4; ++n) {
      acc[0][n] = mfma16(zreg[d][0][1], bfb[n], acc[0][n]);
      acc[1][n] = mfma16(zreg[d][1][1], bfb[n], acc[1][n]);
    }
    __builtin_amdgcn_sched_barrier(0);
    if (it + K3_D < NT) {
#pragma unroll
      for (int k2 = 0; k2 < 2; ++k2) {
        zreg[d][0][k2] = *(const bf16x8*)(zsrc0 + ((it + K3_D) * 4 + 2 * k2) * 512);
        zreg[d][1][k2] = *(const bf16x8*)(zsrc1 + ((it + K3_D) * 4 + 2 * k2) * 512);
      }
    }
    if (it == NT - K3_D) {
#pragma unroll
      for (int n = 0; n < 2; ++n) {
        bsp[n] = bf2f(b_sp[T0 + 16 * (2 * kh + n) + r]);
        zz1[0][n] = *(const u32x2*)(z1 + (size_t)(((tb * 4 + 2 * kh + n) * 56 + ft0) * 64 + lane) * 4);
        zz1[1][n] = *(const u32x2*)(z1 + (size_t)(((tb * 4 + 2 * kh + n) * 56 + ft1) * 64 + lane) * 4);
      }
    }
    __syncthreads();
  }
  STAMP(2, 3);
  {
    f32x4* xch = (f32x4*)smem;
#pragma unroll
    for (int i = 0; i < 2; ++i)
#pragma unroll
      for (int n = 0; n < 2; ++n) xch[((wave * 2 + i) * 2 + n) * 64 + lane] = kh ? acc[i][n] : acc[i][2 + n];
    __syncthreads();
#pragma unroll
    for (int i = 0; i < 2; ++i)
#pragma unroll
      for (int n = 0; n < 2; ++n) {
        const f32x4 o4 = xch[(((wave ^ 4) * 2 + i) * 2 + n) * 64 + lane];
        acc[i][n] = (kh ? acc[i][2 + n] : acc[i][n]) + o4;
      }
  }
  STAMP(2, 4);
  const __amdgpu_buffer_rsrc_t urs = __builtin_amdgcn_make_buffer_rsrc((void*)uout, 0, S_ * FH_ * 2, 0x00020000);
#pragma unroll
  for (int n = 0; n < 2; ++n) {
    const float bb = bsp[n];
#pragma unroll
    for (int i = 0; i < 2; ++i) {
      if (i == 1 && mp == 3) continue;
      const int ftile = i ? ft1 : ft0;
      const u32x2 zz = zz1[i][n];
      float g[4];
#pragma unroll
      for (int j = 0; j < 4; ++j) g[j] = rbf(rbf(acc[i][n][j]) + bb);
      u32x2 o = {pack2(lo2f(zz[0]) * g[0], hi2f(zz[0]) * g[1]), pack2(lo2f(zz[1]) * g[2], hi2f(zz[1]) * g[3])};
      const int off = ((((tb * 4 + 2 * kh + n) * 28 + (ftile >> 1)) * 64 + 16 * (2 * (ftile & 1) + (q >> 1)) + r) * 8 + 4 * (q & 1)) * 2;
      __builtin_amdgcn_raw_buffer_store_b64(o, urs, off, 0, 16);
    }
  }
  asm volatile("s_waitcnt vmcnt(0)" ::: "memory");
  __syncthreads();
  if (tid == 0) __hip_atomic_fetch_add(cnt + tb * 32, 1u, __ATOMIC_RELAXED, __HIP_MEMORY_SCOPE_AGENT);
  STAMP(2, 5);
  if (fb == 7) return;

  bf16x8 wf[14];
#pragma unroll
  for (int k2 = 0; k2 < 14; ++k2)
    wf[k2] = *(const bf16x8*)(w_outT + (size_t)(((fb * 4 + mp) * 28 + 2 * k2 + kh) * 64 + lane) * 8);
  u32x2 hh[2];
#pragma unroll
  for (int n = 0; n < 2; ++n)
    hh[n] = *(const u32x2*)(hin + (size_t)(T0 + 16 * (2 * kh + n) + r) * D_ + fb * 64 + 16 * mp + 4 * q);
  if (tid == 0) {
    for (int spin = 0; spin < (1 << 22); ++spin) {
      const unsigned c = __hip_atomic_load(cnt + tb * 32, __ATOMIC_RELAXED, __HIP_MEMORY_SCOPE_AGENT);
      if (c >= 8u) break;
      __builtin_amdgcn_s_sleep(1);
    }
  }
  __syncthreads();
  STAMP(2, 6);
  {
    u32x4 ul[14];
#pragma unroll
    for (int j = 0; j < 14; ++j)
      ul[j] = __builtin_amdgcn_raw_buffer_load_b128(urs, tb * 64 * FH_ * 2 + (tid + 512 * j) * 16, 0, 16);
#pragma unroll
    for (int j = 0; j < 14; ++j) *(u32x4*)(smem + (tid + 512 * j) * 16) = ul[j];
  }
  __syncthreads();
  STAMP(2, 7);
  f32x4 ac2[4];
#pragma unroll
  for (int n = 0; n < 4; ++n) ac2[n] = (f32x4){0.f, 0.f, 0.f, 0.f};
#pragma unroll
  for (int k2 = 0; k2 < 14; ++k2) {
#pragma unroll
    for (int n = 0; n < 4; ++n) {
      const bf16x8 b = *(const bf16x8*)(smem + ((n * 28 + 2 * k2 + kh) * 64 + lane) * 16);
      ac2[n] = mfma16(wf[k2], b, ac2[n]);
    }
  }
  __syncthreads();
  {
    f32x4* xch = (f32x4*)smem;
#pragma unroll
    for (int n = 0; n < 2; ++n) xch[(wave * 2 + n) * 64 + lane] = kh ? ac2[n] : ac2[2 + n];
    __syncthreads();
#pragma unroll
    for (int n = 0; n < 2; ++n) {
      const f32x4 o4 = (kh ? ac2[2 + n] : ac2[n]) + xch[((wave ^ 4) * 2 + n) * 64 + lane];
      u32x2 o = {pack2(lo2f(hh[n][0]) + rbf(o4[0]), hi2f(hh[n][0]) + rbf(o4[1])),
                 pack2(lo2f(hh[n][1]) + rbf(o4[2]), hi2f(hh[n][1]) + rbf(o4[3]))};
      *(u32x2*)(out + (size_t)(T0 + 16 * (2 * kh + n) + r) * D_ + fb * 64 + 16 * mp + 4 * q) = o;
    }
  }
  STAMP(2, 8);
}

extern "C" void kernel_launch(void* const* d_in, const int* in_sizes, int n_in, void* d_out,
                              int out_size, void* d_ws, size_t ws_size, hipStream_t stream) {
  const u16* xq = (const u16*)d_in[0];
  const u16* kin = (const u16*)d_in[1];
  const u16* vin = (const u16*)d_in[2];
  const u16* shk = (const u16*)d_in[3];
  const u16* ssmk = (const u16*)d_in[4];
  const u16* ssmD = (const u16*)d_in[5];
  const u16* w_in = (const u16*)d_in[7];
  const u16* w_sp = (const u16*)d_in[8];
  const u16* b_sp = (const u16*)d_in[9];
  const u16* w_out = (const u16*)d_in[10];
  char* ws = (char*)d_ws;
  u16* yT = (u16*)(ws + WS_YT);
  u16* h = (u16*)(ws + WS_H);
  u16* w_inT = (u16*)(ws + WS_WINT);
  u16* w_outT = (u16*)(ws + WS_WOUTT);
  float2* ln1 = (float2*)(ws + WS_LN1);
  u16* z1 = (u16*)(ws + WS_Z1);
  u16* z2T = (u16*)(ws + WS_Z2T);
  unsigned long long* pst = (unsigned long long*)(ws + WS_PST);
  u16* kvr = (u16*)(ws + WS_KVR);
  u16* u = (u16*)(ws + WS_U);

  k0_prep<<<256, 512, 0, stream>>>(kin, vin, shk, kvr, xq, w_in, w_out, w_inT, w_outT, ln1, pst);
  k1_conv<<<224, 512, 0, stream>>>(kvr, ssmk, ssmD, yT, w_sp);
  k2_gemm1<<<256, 512, 0, stream>>>(xq, yT, ln1, w_inT, h, z1, z2T, pst);
  k3_gemm2<<<256, 512, 0, stream>>>(w_sp, b_sp, z2T, z1, u, w_outT, h, (u16*)d_out, (unsigned*)(pst + 32 * 512));
}
```

```cpp
#include <hip/hip_runtime.h>
#include <hip/hip_bf16.h>
#include <stdint.h>

typedef __attribute__((ext_vector_type(8))) short bf16x8;
typedef __attribute__((ext_vector_type(4))) short s16x4;
typedef __attribute__((ext_vector_type(4))) float f32x4;
typedef __attribute__((ext_vector_type(2))) float f32x2;
typedef __attribute__((ext_vector_type(4))) unsigned int u32x4;
typedef __attribute__((ext_vector_type(2))) unsigned int u32x2;
typedef __attribute__((ext_vector_type(2))) __bf16 bf16x2_t;
typedef unsigned short u16;

#define DEVI __device__ __forceinline__
#define STAMP(kid, i)

constexpr int S_ = 2048, D_ = 448, F_ = 1792, FH_ = 896;

DEVI float lo2f(unsigned v) { return __uint_as_float(v << 16); }
DEVI float hi2f(unsigned v) { return __uint_as_float(v & 0xffff0000u); }
DEVI float bf2f(u16 v) { return __uint_as_float(((unsigned)v) << 16); }
DEVI unsigned pack2(float a, float b) {
  f32x2 f = {a, b};
  bf16x2_t h = __builtin_convertvector(f, bf16x2_t);
  return __builtin_bit_cast(unsigned, h);
}
DEVI float rbf(float x) { return lo2f(pack2(x, x)); }
DEVI f32x4 mfma16(bf16x8 a, bf16x8 b, f32x4 c) {
  return __builtin_amdgcn_mfma_f32_16x16x32_bf16(a, b, c, 0, 0, 0);
}
template <int CTRL> DEVI float dpp_f(float x) {
  return __builtin_bit_cast(float, __builtin_amdgcn_update_dpp(0, __builtin_bit_cast(int, x), CTRL, 0xf, 0xf, false));
}
DEVI float row16_sum(float x) {
  x += dpp_f<0x121>(x);
  x += dpp_f<0x122>(x);
  x += dpp_f<0x124>(x);
  x += dpp_f<0x128>(x);
  return x;
}
DEVI float gelu_tanh(float x) {
  const float u = x * (0.7978845608028654f + 0.035677408136300125f * x * x);
  const float e = __builtin_amdgcn_exp2f(-2.885390081777927f * u);
  return x * __builtin_amdgcn_rcpf(1.0f + e);
}

constexpr size_t WS_YT = 0;
constexpr size_t WS_H = WS_YT + (size_t)D_ * S_ * 2;
constexpr size_t WS_WINT = WS_H + (size_t)S_ * D_ * 2;
constexpr size_t WS_WOUTT = WS_WINT + (size_t)F_ * D_ * 2;
constexpr size_t WS_LN1 = WS_WOUTT + (size_t)D_ * FH_ * 2;
constexpr size_t WS_Z1 = WS_LN1 + (size_t)S_ * 8;
constexpr size_t WS_Z2T = WS_Z1 + (size_t)S_ * FH_ * 2;
constexpr size_t WS_PST = WS_Z2T + (size_t)S_ * FH_ * 2;
constexpr size_t WS_U = WS_PST + (size_t)9 * S_ * 8;
constexpr size_t WS_KVR = WS_U + (size_t)S_ * FH_ * 2;

constexpr int K1_XREV = 0;
constexpr int K1_XSTRIDE = 2304 * 2;
constexpr int K1_COP = 9216;
constexpr int K1_CSTRIDE = 4128;
constexpr int K1_RED = K1_COP + 2 * 8 * K1_CSTRIDE;
constexpr int K1_LDS = K1_RED + 8 * 8192;

DEVI float wave_sum(float x) {
  x = row16_sum(x);
  const int xi = __builtin_bit_cast(int, x);
  return __builtin_bit_cast(float, __builtin_amdgcn_readlane(xi, 0)) + __builtin_bit_cast(float, __builtin_amdgcn_readlane(xi, 16)) +
         __builtin_bit_cast(float, __builtin_amdgcn_readlane(xi, 32)) + __builtin_bit_cast(float, __builtin_amdgcn_readlane(xi, 48));
}

__global__ __launch_bounds__(512) void k0_prep(
    const u16* __restrict__ kin, const u16* __restrict__ vin, const u16* __restrict__ shk,
    u16* __restrict__ kvr,
    const u16* __restrict__ xin, const u16* __restrict__ w_in, const u16* __restrict__ w_out,
    u16* __restrict__ w_inT, u16* __restrict__ w_outT, float2* __restrict__ ln1,
    unsigned long long* __restrict__ gran, const u16* __restrict__ ssmk) {
  __shared__ __attribute__((aligned(16))) char smem[8 * 4608 + 64 * 144];
  const int tid = threadIdx.x, lane = tid & 63;
  const int wave = __builtin_amdgcn_readfirstlane(tid >> 6);
  const int blk = blockIdx.x;
  STAMP(4, 0);
  const int gw = blk * 8 + wave;
  const int r = lane & 15, q = lane >> 4;

  const bool has_tile = blk < 224;
  const int S0 = (blk / 7) * 64, D0 = (blk % 7) * 64;
  const int row = tid >> 3, c8 = (tid & 7) * 8;
  u32x4 kq[4], vq = {0u, 0u, 0u, 0u}, sk[4];
  if (has_tile) {
    const int s = S0 + row;
#pragma unroll
    for (int j = 0; j < 4; ++j) {
      kq[j] = (u32x4){0u, 0u, 0u, 0u};
      if (s - 3 + j >= 0) kq[j] = *(const u32x4*)(kin + (size_t)(s - 3 + j) * D_ + D0 + c8);
    }
    vq = *(const u32x4*)(vin + (size_t)s * D_ + D0 + c8);
#pragma unroll
    for (int j = 0; j < 4; ++j) sk[j] = *(const u32x4*)(shk + (D0 + c8) * 4 + j * 8);
  }
  u32x4 xr = {0u, 0u, 0u, 0u};
  if (lane < 56) xr = *(const u32x4*)(xin + (size_t)gw * D_ + lane * 8);
  const bool has_slab = gw < 588;
  u32x4 sl[4];
  if (has_slab) {
    const u16* src; int ld;
    if (gw < 392) { src = w_in + (size_t)(gw / 28) * 32 * F_ + (gw % 28) * 64; ld = F_; }
    else { const int i2 = gw - 392; src = w_out + (size_t)(i2 / 7) * 32 * D_ + (i2 % 7) * 64; ld = D_; }
#pragma unroll
    for (int i = 0; i < 4; ++i)
      sl[i] = *(const u32x4*)(src + (size_t)(8 * i + (lane >> 3)) * ld + (lane & 7) * 8);
  }
  const u32x4 pf = ((const u32x4*)ssmk)[blk < 224 ? blk * 512 + tid : tid];
  if (blk < 33) gran[blk * 512 + tid] = 0ull;

  STAMP(4, 1);
  u16* tl = (u16*)(smem + 8 * 4608);
  if (has_tile) {
#pragma unroll
    for (int e = 0; e < 8; ++e) {
      const unsigned s01 = sk[e >> 1][(e & 1) * 2], s23 = sk[e >> 1][(e & 1) * 2 + 1];
      float kf[4];
#pragma unroll
      for (int j = 0; j < 4; ++j) kf[j] = (e & 1) ? hi2f(kq[j][e >> 1]) : lo2f(kq[j][e >> 1]);
      float a = rbf(kf[0] * hi2f(s23));
      a = rbf(a + rbf(kf[1] * lo2f(s23)));
      a = rbf(a + rbf(kf[2] * hi2f(s01)));
      a = rbf(a + rbf(kf[3] * lo2f(s01)));
      const float vf = (e & 1) ? hi2f(vq[e >> 1]) : lo2f(vq[e >> 1]);
      tl[(c8 + e) * 72 + (63 - row)] = (u16)(pack2(a * vf, 0.f) & 0xffffu);
    }
  }
  STAMP(4, 2);
  {
    float v[8];
#pragma unroll
    for (int j = 0; j < 4; ++j) { v[2 * j] = lo2f(xr[j]); v[2 * j + 1] = hi2f(xr[j]); }
    float sm = 0.f;
#pragma unroll
    for (int j = 0; j < 8; ++j) sm += v[j];
    const float mean = wave_sum(sm) * (1.0f / D_);
    float m2 = 0.f;
    if (lane < 56) {
#pragma unroll
      for (int j = 0; j < 8; ++j) { float d = v[j] - mean; m2 += d * d; }
    }
    m2 = wave_sum(m2);
    if (lane == 0) {
      const float var = rbf(m2 * (1.0f / D_));
      const float sd = rbf(sqrtf(rbf(var + 1e-5f)));
      ln1[gw] = make_float2(rbf(mean), 1.0f / sd);
    }
  }
  STAMP(4, 3);
  if (has_slab) {
    typedef __attribute__((address_space(3))) s16x4* lp;
    char* ts = smem + wave * 4608;
    u16* dst; int bstride;
    if (gw < 392) { dst = w_inT + (size_t)((gw % 28) * 4 * 14 + (gw / 28)) * 512; bstride = 14 * 512; }
    else { const int i2 = gw - 392; dst = w_outT + (size_t)((i2 % 7) * 4 * 28 + (i2 / 7)) * 512; bstride = 28 * 512; }
#pragma unroll
    for (int i = 0; i < 4; ++i) *(u32x4*)(ts + (8 * i + (lane >> 3)) * 144 + (lane & 7) * 16) = sl[i];
#pragma unroll
    for (int n = 0; n < 4; ++n) {
      const char* a0p = ts + (8 * q + (r >> 2)) * 144 + (16 * n + 4 * (r & 3)) * 2;
      s16x4 v0 = __builtin_amdgcn_ds_read_tr16_b64_v4i16((lp)(a0p));
      s16x4 v1 = __builtin_amdgcn_ds_read_tr16_b64_v4i16((lp)(a0p + 4 * 144));
      bf16x8 o = {v0[0], v0[1], v0[2], v0[3], v1[0], v1[1], v1[2], v1[3]};
      *(bf16x8*)(dst + (size_t)n * bstride + lane * 8) = o;
    }
  }
  STAMP(4, 4);
  if (has_tile) {
    __syncthreads();
    const u32x4 o = *(const u32x4*)(tl + row * 72 + c8);
    *(u32x4*)(kvr + (size_t)(D0 + row) * S_ + (1984 - S0) + c8) = o;
  }
  asm volatile("" ::"v"(pf));
  STAMP(4, 5);
}

__global__ __launch_bounds__(512) void k1_conv(
    const u16* __restrict__ kvr, const u16* __restrict__ ssmk, const u16* __restrict__ ssmD,
    u16* __restrict__ yT, const u16* __restrict__ w_sp) {
  __shared__ __attribute__((aligned(16))) char smem[K1_LDS];
  const int tid = threadIdx.x, lane = tid & 63, wave = tid >> 6;
  STAMP(0, 0);
  const int xcd = blockIdx.x & 7, idx = blockIdx.x >> 3;
  const int d0 = (xcd * 28 + idx) * 2;
  const int tch = tid >> 8, mm = tid & 255;
  const u16* krow = ssmk + (size_t)(d0 + tch) * S_;
  const u32x4 xr = *(const u32x4*)(kvr + (size_t)(d0 + tch) * S_ + mm * 8);
  u32x4 wa = *(const u32x4*)(krow + mm * 8);
  u32x4 wb = {0u, 0u, 0u, 0u};
  if (mm < 255) wb = *(const u32x4*)(krow + mm * 8 + 8);
  const unsigned dDw = *(const unsigned*)(ssmD + d0);
  *(u32x4*)(smem + K1_XREV + tch * K1_XSTRIDE + mm * 16) = xr;

  if (tid < 64) {
    u32x4 z = {0u, 0u, 0u, 0u};
    *(u32x4*)(smem + K1_XREV + (tid >> 5) * K1_XSTRIDE + (2048 + (tid & 31) * 8) * 2) = z;
  }
  {
    unsigned w[8] = {wa[0], wa[1], wa[2], wa[3], wb[0], wb[1], wb[2], wb[3]};
    unsigned al[7];
#pragma unroll
    for (int i = 0; i < 7; ++i) al[i] = (w[i] >> 16) | (w[i + 1] << 16);
    char* base = smem + K1_COP + tch * 8 * K1_CSTRIDE + (mm + 2) * 16;
#pragma unroll
    for (int c = 0; c < 8; ++c) {
      u32x4 o;
      if ((c & 1) == 0) { o[0] = w[c / 2]; o[1] = w[c / 2 + 1]; o[2] = w[c / 2 + 2]; o[3] = w[c / 2 + 3]; }
      else { o[0] = al[c / 2]; o[1] = al[c / 2 + 1]; o[2] = al[c / 2 + 2]; o[3] = al[c / 2 + 3]; }
      *(u32x4*)(base + c * K1_CSTRIDE) = o;
    }
    if (mm < 2) {
      unsigned w2[8] = {0u, 0u, 0u, 0u, 0u, 0u, 0u, 0u};
      if (mm == 0) { w2[4] = wa[0]; w2[5] = wa[1]; w2[6] = wa[2]; w2[7] = wa[3]; }
      unsigned a2[7];
#pragma unroll
      for (int i = 0; i < 7; ++i) a2[i] = (w2[i] >> 16) | (w2[i + 1] << 16);
      char* b2 = smem + K1_COP + tch * 8 * K1_CSTRIDE + (1 - mm) * 16;
#pragma unroll
      for (int c = 0; c < 8; ++c) {
        u32x4 o;
        if ((c & 1) == 0) { o[0] = w2[c / 2]; o[1] = w2[c / 2 + 1]; o[2] = w2[c / 2 + 2]; o[3] = w2[c / 2 + 3]; }
        else { o[0] = a2[c / 2]; o[1] = a2[c / 2 + 1]; o[2] = a2[c / 2 + 2]; o[3] = a2[c / 2 + 3]; }
        *(u32x4*)(b2 + c * K1_CSTRIDE) = o;
      }
    }
  }
  STAMP(0, 1);
  __syncthreads();
  STAMP(0, 2);

  u32x4 pf[5];
  {
    const int base = (xcd * 28 + idx) * 2560 + tid;
#pragma unroll
    for (int j = 0; j < 5; ++j) {
      const int c = base + j * 512;
      pf[j] = ((const u32x4*)w_sp)[c < S_ * S_ / 8 ? c : 0];
    }
  }
  {
    const int ch = wave & 1, r = lane & 15, q = lane >> 4;
    f32x4 acc[8];
#pragma unroll
    for (int t = 0; t < 8; ++t) acc[t] = (f32x4){0.f, 0.f, 0.f, 0.f};
    const char* abase = smem + K1_COP + ch * 8 * K1_CSTRIDE + ((r + 1) & 7) * K1_CSTRIDE +
                        (2 * (q >> 1) - (q & 1) + ((r + 9) >> 3)) * 16;
    const char* bbase = smem + K1_XREV + ch * K1_XSTRIDE + (2040 - 16 * r + 16 * (q >> 1) - 8 * (q & 1)) * 2;
#pragma unroll
    for (int cc = 0; cc < 2; ++cc) {
      const int rho = 2 * (wave >> 1) + cc;
      bf16x8 A[8], B[8];
#pragma unroll
      for (int i = 0; i < 8; ++i) A[i] = *(const bf16x8*)(abase + (rho + 8 * i) * 64);
#pragma unroll
      for (int i = 0; i < 8; ++i) B[i] = *(const bf16x8*)(bbase - (8 * i - rho) * 64);
#pragma unroll
      for (int t = 0; t < 8; ++t)
#pragma unroll
        for (int i = 0; i <= t; ++i) acc[t] = mfma16(A[i], B[t - i], acc[t]);
    }
    float* red = (float*)(smem + K1_RED + wave * 8192);
#pragma unroll
    for (int t = 0; t < 8; ++t) *(f32x4*)(red + 256 * t + 16 * r + 4 * q) = acc[t];
  }
  STAMP(0, 3);
  __syncthreads();
  STAMP(0, 4);
  {
    const int ch = tid >> 8, e0 = (tid & 255) * 8;
    f32x4 sa = {0.f, 0.f, 0.f, 0.f}, sb = {0.f, 0.f, 0.f, 0.f};
#pragma unroll
    for (int w = 0; w < 4; ++w) {
      const float* red = (const float*)(smem + K1_RED + (2 * w + ch) * 8192);
      sa += *(const f32x4*)(red + e0);
      sb += *(const f32x4*)(red + e0 + 4);
    }
    const u32x4 kr = *(const u32x4*)(smem + K1_XREV + ch * K1_XSTRIDE + (2040 - e0) * 2);
    const float Dv = ch ? hi2f(dDw) : lo2f(dDw);
    float kvf[8];
    kvf[7] = lo2f(kr[0]); kvf[6] = hi2f(kr[0]); kvf[5] = lo2f(kr[1]); kvf[4] = hi2f(kr[1]);
    kvf[3] = lo2f(kr[2]); kvf[2] = hi2f(kr[2]); kvf[1] = lo2f(kr[3]); kvf[0] = hi2f(kr[3]);
    float y[8];
#pragma unroll
    for (int j = 0; j < 4; ++j) {
      y[j] = rbf(sa[j]) + rbf(kvf[j] * Dv);
      y[j + 4] = rbf(sb[j]) + rbf(kvf[j + 4] * Dv);
    }
    u32x4 o = {pack2(y[0], y[1]), pack2(y[2], y[3]), pack2(y[4], y[5]), pack2(y[6], y[7])};
    *(u32x4*)(yT + (size_t)(d0 + ch) * S_ + e0) = o;
  }
#pragma unroll
  for (int j = 0; j < 5; ++j) asm volatile("" ::"v"(pf[j]));
  STAMP(0, 5);
}

constexpr int K2_YT = 0;
constexpr int K2_AT = 448 * 80;
constexpr int K2_ST = K2_AT + 32 * 1024;
constexpr int K2_FS = K2_ST + 8 * 32 * 8;
constexpr int K2_LDS = K2_FS + 32 * 8;

DEVI f32x2 unp(unsigned v) { return (f32x2){lo2f(v), hi2f(v)}; }
DEVI unsigned pk(f32x2 v) { return pack2(v[0], v[1]); }

__global__ __launch_bounds__(512) void k2_gemm1(
    const u16* __restrict__ xin, const u16* __restrict__ yT, const float2* __restrict__ ln1,
    const u16* __restrict__ w_inT, u16* __restrict__ hout, u16* __restrict__ z1,
    u16* __restrict__ z2T, unsigned long long* pstat) {
  __shared__ __attribute__((aligned(16))) char smem[K2_LDS];
  const int tid = threadIdx.x, lane = tid & 63;
  const int wave = __builtin_amdgcn_readfirstlane(tid >> 6);
  STAMP(1, 0);
  const int xcd = blockIdx.x & 7, idx = blockIdx.x >> 3;
  const int rb = xcd * 8 + (idx & 7), cb = idx >> 3;
  const int R0 = rb * 32;
  const int r = lane & 15, q = lane >> 4;

  u32x4 yl[4], xg[4];
#pragma unroll
  for (int j = 0; j < 4; ++j) {
    const int id = tid + 512 * j;
    if (j < 3 || tid < 256) {
      yl[j] = *(const u32x4*)(yT + (size_t)(id >> 2) * S_ + R0 + (id & 3) * 8);
      xg[j] = *(const u32x4*)(xin + (size_t)R0 * D_ + id * 8);
    }
  }
  const int prow = 16 * (wave & 1) + r;
  const int dq0 = (wave >> 1) * 28 + q;
  const float2 l1 = ln1[R0 + prow];
  const int wv = wave < 7 ? wave : 6;
  const u16* wz2 = w_inT + (size_t)((56 + cb * 14 + 2 * wv) * 14 * 64 + lane) * 8;
  const u16* wz1 = w_inT + (size_t)((cb * 14 + 2 * wv) * 14 * 64 + lane) * 8;
  bf16x8 wf[2][14];
#pragma unroll
  for (int ks = 0; ks < 7; ++ks)
#pragma unroll
    for (int i = 0; i < 2; ++i) wf[i][ks] = *(const bf16x8*)(wz2 + (i * 14 + ks) * 512);
#pragma unroll
  for (int j = 0; j < 4; ++j) {
    const int id = tid + 512 * j;
    if (j < 3 || tid < 256) {
      *(u32x4*)(smem + K2_YT + (id >> 2) * 80 + (id & 3) * 16) = yl[j];
      const int row = id / 56, c = id % 56;
      *(u32x4*)(smem + K2_AT + row * 1024 + ((c ^ (row & 15)) * 16)) = xg[j];
    }
  }
  STAMP(1, 1);
  __syncthreads();
  STAMP(1, 2);
  f32x2 hv[7][2];
  f32x2 sum2 = {0.f, 0.f};
  {
    typedef __attribute__((address_space(3))) s16x4* lp;
    const int qq = r >> 2, pp = r & 3;
    const f32x2 m1 = {l1.x, l1.x}, r1 = {l1.y, l1.y};
#pragma unroll
    for (int it = 0; it < 7; ++it) {
      const int dq = dq0 + 4 * it;
      const char* a0 = smem + K2_YT + (4 * dq + qq) * 80 + (16 * (wave & 1) + 4 * pp) * 2;
      const u32x2 y0 = __builtin_bit_cast(u32x2, __builtin_amdgcn_ds_read_tr16_b64_v4i16((lp)(a0)));
      const u32x2 xv = *(const u32x2*)(smem + K2_AT + prow * 1024 + (((dq >> 1) ^ r) * 16) + (dq & 1) * 8);
#pragma unroll
      for (int j = 0; j < 2; ++j) {
        const f32x2 x2 = unp(xv[j]);
        const unsigned dd = pk(x2 - m1);
        const unsigned qn = pk(unp(dd) * r1);
        const unsigned aa = pk(unp(qn) * unp(y0[j]));
        hv[it][j] = unp(pk(x2 + unp(aa)));
        sum2 += hv[it][j];
      }
#pragma unroll
      for (int i = 0; i < 2; ++i) wf[i][7 + it] = *(const bf16x8*)(wz2 + (i * 14 + 7 + it) * 512);
      __builtin_amdgcn_sched_barrier(0);
    }
  }
  float sum = sum2[0] + sum2[1];
  sum += __shfl_xor(sum, 16);
  sum += __shfl_xor(sum, 32);
  const float mh = sum * (1.0f / 112.0f);
  f32x2 q2 = {0.f, 0.f};
  {
    const f32x2 mh2 = {mh, mh};
#pragma unroll
    for (int it = 0; it < 7; ++it)
#pragma unroll
      for (int j = 0; j < 2; ++j) {
        const f32x2 d = hv[it][j] - mh2;
        q2 += d * d;
      }
  }
  float m2 = q2[0] + q2[1];
  m2 += __shfl_xor(m2, 16);
  m2 += __shfl_xor(m2, 32);
  float2* st = (float2*)(smem + K2_ST);
  if (q == 0) st[wave * 16 + r] = make_float2(mh, m2);
  STAMP(1, 3);
  __syncthreads();
  STAMP(1, 4);
  {
    float2 pq[4];
    float ms = 0.f;
#pragma unroll
    for (int w = 0; w < 4; ++w) { pq[w] = st[((wave & 1) + 2 * w) * 16 + r]; ms += pq[w].x; }
    const float mean = 0.25f * ms;
    float M2 = 0.f;
#pragma unroll
    for (int w = 0; w < 4; ++w) { const float dl = pq[w].x - mean; M2 += pq[w].y + 112.0f * dl * dl; }
    const float var = rbf(M2 * (1.0f / D_));
    const float mb = rbf(mean);
    const float rinv = 1.0f / rbf(sqrtf(rbf(var + 1e-5f)));
    const f32x2 mb2 = {mb, mb}, ri2 = {rinv, rinv};
#pragma unroll
    for (int it = 0; it < 7; ++it) {
      u32x2 o2;
#pragma unroll
      for (int j = 0; j < 2; ++j) o2[j] = pk(unp(pk(hv[it][j] - mb2)) * ri2);
      const int dq = dq0 + 4 * it;
      *(u32x2*)(smem + K2_AT + prow * 1024 + (((dq >> 1) ^ r) * 16) + (dq & 1) * 8) = o2;
      if (cb == 0) {
        u32x2 h2 = {pk(hv[it][0]), pk(hv[it][1])};
        *(u32x2*)(hout + (size_t)(R0 + prow) * D_ + dq * 4) = h2;
      }
    }
  }
  STAMP(1, 5);
  __syncthreads();
  STAMP(1, 6);

  u32x2 zp[2][2];
  bf16x8 wg[2][14];
  if (wave < 7) {
    f32x4 acc[2][2];
#pragma unroll
    for (int i = 0; i < 2; ++i)
#pragma unroll
      for (int s = 0; s < 2; ++s) acc[i][s] = (f32x4){0.f, 0.f, 0.f, 0.f};
#pragma unroll
    for (int ks = 0; ks < 14; ++ks) {
      bf16x8 xf[2];
#pragma unroll
      for (int s = 0; s < 2; ++s)
        xf[s] = *(const bf16x8*)(smem + K2_AT + (16 * s + r) * 1024 + (((4 * ks + q) ^ r) * 16));
#pragma unroll
      for (int i = 0; i < 2; ++i)
#pragma unroll
        for (int s = 0; s < 2; ++s) acc[i][s] = mfma16(xf[s], wf[i][ks], acc[i][s]);
#pragma unroll
      for (int i = 0; i < 2; ++i) wg[i][ks] = *(const bf16x8*)(wz1 + (i * 14 + ks) * 512);
    }
#pragma unroll
    for (int s = 0; s < 2; ++s) {
#pragma unroll
      for (int i = 0; i < 2; ++i) {
        float zz[4];
#pragma unroll
        for (int j = 0; j < 4; ++j) zz[j] = gelu_tanh(rbf(acc[i][s][j]));
        zp[i][s] = (u32x2){pack2(zz[0], zz[1]), pack2(zz[2], zz[3])};
      }
      float mw[4], qw[4];
#pragma unroll
      for (int j = 0; j < 4; ++j) {
        const float v0 = (j & 1) ? hi2f(zp[0][s][j >> 1]) : lo2f(zp[0][s][j >> 1]);
        const float v1 = (j & 1) ? hi2f(zp[1][s][j >> 1]) : lo2f(zp[1][s][j >> 1]);
        mw[j] = row16_sum(v0 + v1) * (1.0f / 32.0f);
        const float a0 = v0 - mw[j], a1 = v1 - mw[j];
        qw[j] = row16_sum(a0 * a0 + a1 * a1);
      }
      if (r == 0) {
#pragma unroll
        for (int j = 0; j < 4; ++j) st[wave * 32 + 16 * s + 4 * q + j] = make_float2(mw[j], qw[j]);
      }
    }
  }
  __syncthreads();
  if (wave == 7) {
    float2* fs = (float2*)(smem + K2_FS);
    const int row = lane >> 1;
    float msum = 0.f;
    float2 pw[7];
#pragma unroll
    for (int w = 0; w < 7; ++w) { pw[w] = st[w * 32 + row]; msum += pw[w].x; }
    const float bmean = msum * (1.0f / 7.0f);
    float bM2 = 0.f;
#pragma unroll
    for (int w = 0; w < 7; ++w) { const float dl = pw[w].x - bmean; bM2 += pw[w].y + 32.0f * dl * dl; }
    const float mine = (lane & 1) ? bM2 : bmean;
    unsigned long long* g = pstat + (size_t)rb * 256 + lane;
    __hip_atomic_store(g + cb * 64, (1ull << 32) | __float_as_uint(mine), __ATOMIC_RELAXED, __HIP_MEMORY_SCOPE_AGENT);
    float pv[4];
    for (int spin = 0; spin < (1 << 22); ++spin) {
      bool ok = true;
#pragma unroll
      for (int c = 0; c < 4; ++c) {
        const unsigned long long v = __hip_atomic_load(g + c * 64, __ATOMIC_RELAXED, __HIP_MEMORY_SCOPE_AGENT);
        pv[c] = __uint_as_float((unsigned)v);
        ok &= (v >> 32) == 1ull;
      }
      if (__all(ok)) break;
      __builtin_amdgcn_s_sleep(1);
    }
    float pm[4], pq[4];
#pragma unroll
    for (int c = 0; c < 4; ++c) {
      const float o = __shfl_xor(pv[c], 1);
      pm[c] = (lane & 1) ? o : pv[c];
      pq[c] = (lane & 1) ? pv[c] : o;
    }
    const float mean = (pm[0] + pm[1] + pm[2] + pm[3]) * 0.25f;
    float M2 = 0.f;
#pragma unroll
    for (int c = 0; c < 4; ++c) { const float dl = pm[c] - mean; M2 += pq[c] + 224.0f * dl * dl; }
    const float var = rbf(M2 * (1.0f / FH_));
    if ((lane & 1) == 0) fs[row] = make_float2(rbf(mean), 1.0f / rbf(sqrtf(rbf(var + 1e-5f))));
  } else {
    STAMP(1, 7);
    f32x4 acc[2][2];
#pragma unroll
    for (int i = 0; i < 2; ++i)
#pragma unroll
      for (int s = 0; s < 2; ++s) acc[i][s] = (f32x4){0.f, 0.f, 0.f, 0.f};
#pragma unroll
    for (int ks = 0; ks < 14; ++ks) {
      bf16x8 xf[2];
#pragma unroll
      for (int s = 0; s < 2; ++s)
        xf[s] = *(const bf16x8*)(smem + K2_AT + (16 * s + r) * 1024 + (((4 * ks + q) ^ r) * 16));
#pragma unroll
      for (int i = 0; i < 2; ++i)
#pragma unroll
        for (int s = 0; s < 2; ++s) acc[i][s] = mfma16(wg[i][ks], xf[s], acc[i][s]);
    }
#pragma unroll
    for (int s = 0; s < 2; ++s)
#pragma unroll
      for (int i = 0; i < 2; ++i) {
        float zz[4];
#pragma unroll
        for (int j = 0; j < 4; ++j) zz[j] = gelu_tanh(rbf(acc[i][s][j]));
        u32x2 o = {pack2(zz[0], zz[1]), pack2(zz[2], zz[3])};
        *(u32x2*)(z1 + (size_t)(((rb * 2 + s) * 56 + cb * 14 + 2 * wave + i) * 64 + lane) * 4) = o;
      }
    STAMP(1, 8);
  }
  __syncthreads();
  STAMP(1, 9);
  if (wave < 7) {
    const float2* fs = (const float2*)(smem + K2_FS);
#pragma unroll
    for (int s = 0; s < 2; ++s) {
      const f32x4 sa = *(const f32x4*)(fs + 16 * s + 4 * q), sb = *(const f32x4*)(fs + 16 * s + 4 * q + 2);
#pragma unroll
      for (int i = 0; i < 2; ++i) {
        const float n0 = rbf(lo2f(zp[i][s][0]) - sa[0]) * sa[1], n1 = rbf(hi2f(zp[i][s][0]) - sa[2]) * sa[3];
        const float n2 = rbf(lo2f(zp[i][s][1]) - sb[0]) * sb[1], n3 = rbf(hi2f(zp[i][s][1]) - sb[2]) * sb[3];
        u32x2 o = {pack2(n0, n1), pack2(n2, n3)};
        const int ftile = cb * 14 + 2 * wave + i;
        const int lq = 2 * s + (q >> 1);
        *(u32x2*)(z2T + (size_t)((ftile * 64 + rb) * 64 + 16 * lq + r) * 8 + 4 * (q & 1)) = o;
      }
    }
  }
  STAMP(1, 10);
}

constexpr int K3_WB = 0;
constexpr int K3_LDS = 32 * 1792;
constexpr int K3_D = 4;

__global__ __launch_bounds__(512) void k3_gemm2(
    const u16* __restrict__ w_sp, const u16* __restrict__ b_sp, const u16* __restrict__ z2T,
    const u16* __restrict__ z1, u16* uout, const u16* __restrict__ w_outT,
    const u16* __restrict__ hin, u16* __restrict__ out, unsigned* cnt) {
  __shared__ __attribute__((aligned(16))) char smem[K3_LDS];
  const int tid = threadIdx.x, lane = tid & 63;
  const int wave = __builtin_amdgcn_readfirstlane(tid >> 6);
  STAMP(2, 0);
  const int xcd = blockIdx.x & 7, idx = blockIdx.x >> 3;
  const int tb = (xcd & 3) * 8 + (idx & 7), fb = (xcd >> 2) * 4 + (idx >> 3);
  const int T0 = tb * 64;
  const int r = lane & 15, q = lane >> 4;
  const int kh = wave >> 2, mp = wave & 3;
  constexpr int NT = 16;

  const u16* wsrc = w_sp + (size_t)(T0 + (tid >> 4)) * S_ + (tid & 15) * 8;
  const int ft0 = fb * 7 + 2 * mp, ft1 = fb * 7 + (mp < 3 ? 2 * mp + 1 : 2 * mp);
  const u16* zsrc0 = z2T + (size_t)((ft0 * 64 + kh) * 64 + lane) * 8;
  const u16* zsrc1 = z2T + (size_t)((ft1 * 64 + kh) * 64 + lane) * 8;
  const int wdst = (tid >> 4) * 256 + (((tid & 15) ^ ((tid >> 4) & 15)) * 16);

  u32x4 wreg[K3_D][2];
  bf16x8 zreg[K3_D][2][2];
  auto ldtile = [&](int d) {
#pragma unroll
    for (int j = 0; j < 2; ++j) wreg[d][j] = *(const u32x4*)(wsrc + (size_t)(32 * j) * S_ + d * 128);
#pragma unroll
    for (int k2 = 0; k2 < 2; ++k2) {
      zreg[d][0][k2] = *(const bf16x8*)(zsrc0 + (d * 4 + 2 * k2) * 512);
      zreg[d][1][k2] = *(const bf16x8*)(zsrc1 + (d * 4 + 2 * k2) * 512);
    }
  };
  ldtile(0);
  ldtile(1);
  auto stageW = [&](int it) {
    const int d = it % K3_D;
#pragma unroll
    for (int j = 0; j < 2; ++j) *(u32x4*)(smem + K3_WB + (it % 3) * 16384 + j * 8192 + wdst) = wreg[d][j];
    if (it + K3_D < NT) {
#pragma unroll
      for (int j = 0; j < 2; ++j) wreg[d][j] = *(const u32x4*)(wsrc + (size_t)(32 * j) * S_ + (it + K3_D) * 128);
    }
  };
  auto ldfrag = [&](bf16x8 (&bf)[4], int it, int k2) {
#pragma unroll
    for (int n = 0; n < 4; ++n)
      bf[n] = *(const bf16x8*)(smem + K3_WB + (it % 3) * 16384 + (16 * n + r) * 256 + (((4 * (2 * k2 + kh) + q) ^ r) * 16));
  };
  __builtin_amdgcn_sched_barrier(0);
  ldtile(2);
  __builtin_amdgcn_sched_barrier(0);
  stageW(0);
  __builtin_amdgcn_sched_barrier(0);
  ldtile(3);
  __builtin_amdgcn_sched_barrier(0);
  stageW(1);
  STAMP(2, 1);
  __syncthreads();
  STAMP(2, 2);

  f32x4 acc[2][4];
#pragma unroll
  for (int i = 0; i < 2; ++i)
#pragma unroll
    for (int n = 0; n < 4; ++n) acc[i][n] = (f32x4){0.f, 0.f, 0.f, 0.f};
  u32x2 zz1[2][2];
  float bsp[2];
  bf16x8 bfa[4], bfb[4];
  const int th = fb >> 2, dq = fb & 3;
  const int wv = wave < 7 ? wave : 6;
  bf16x8 wf[28];
  ldfrag(bfa, 0, 0);
#pragma unroll
  for (int it = 0; it < NT; ++it) {
    const int d = it % K3_D;
    ldfrag(bfb, it, 1);
    if (it + 2 < NT) stageW(it + 2);
    __builtin_amdgcn_sched_barrier(0);
#pragma unroll
    for (int n = 0; n < 4; ++n) {
      acc[0][n] = mfma16(zreg[d][0][0], bfa[n], acc[0][n]);
      acc[1][n] = mfma16(zreg[d][1][0], bfa[n], acc[1][n]);
    }
    __builtin_amdgcn_sched_barrier(0);
    if (it + 1 < NT) ldfrag(bfa, it + 1, 0);
    __builtin_amdgcn_sched_barrier(0);
#pragma unroll
    for (int n = 0; n < 4; ++n) {
      acc[0][n] = mfma16(zreg[d][0][1], bfb[n], acc[0][n]);
      acc[1][n] = mfma16(zreg[d][1][1], bfb[n], acc[1][n]);
    }
    __builtin_amdgcn_sched_barrier(0);
    if (it + K3_D < NT) {
#pragma unroll
      for (int k2 = 0; k2 < 2; ++k2) {
        zreg[d][0][k2] = *(const bf16x8*)(zsrc0 + ((it + K3_D) * 4 + 2 * k2) * 512);
        zreg[d][1][k2] = *(const bf16x8*)(zsrc1 + ((it + K3_D) * 4 + 2 * k2) * 512);
      }
    }
    if (it >= NT - 4) {
#pragma unroll
      for (int k7 = 0; k7 < 7; ++k7) {
        const int ks = 7 * (it - (NT - 4)) + k7;
        wf[ks] = *(const bf16x8*)(w_outT + (size_t)(((dq * 7 + wv) * 28 + ks) * 64 + lane) * 8);
      }
    }
    if (it == NT - K3_D) {
#pragma unroll
      for (int n = 0; n < 2; ++n) {
        bsp[n] = bf2f(b_sp[T0 + 16 * (2 * kh + n) + r]);
        zz1[0][n] = *(const u32x2*)(z1 + (size_t)(((tb * 4 + 2 * kh + n) * 56 + ft0) * 64 + lane) * 4);
        zz1[1][n] = *(const u32x2*)(z1 + (size_t)(((tb * 4 + 2 * kh + n) * 56 + ft1) * 64 + lane) * 4);
      }
    }
    __syncthreads();
  }
  STAMP(2, 3);
  {
    f32x4* xch = (f32x4*)smem;
#pragma unroll
    for (int i = 0; i < 2; ++i)
#pragma unroll
      for (int n = 0; n < 2; ++n) xch[((wave * 2 + i) * 2 + n) * 64 + lane] = kh ? acc[i][n] : acc[i][2 + n];
    __syncthreads();
#pragma unroll
    for (int i = 0; i < 2; ++i)
#pragma unroll
      for (int n = 0; n < 2; ++n) {
        const f32x4 o4 = xch[(((wave ^ 4) * 2 + i) * 2 + n) * 64 + lane];
        acc[i][n] = (kh ? acc[i][2 + n] : acc[i][n]) + o4;
      }
  }
  STAMP(2, 4);
  const __amdgpu_buffer_rsrc_t urs = __builtin_amdgcn_make_buffer_rsrc((void*)uout, 0, S_ * FH_ * 2, 0x00020000);
#pragma unroll
  for (int n = 0; n < 2; ++n) {
    const float bb = bsp[n];
#pragma unroll
    for (int i = 0; i < 2; ++i) {
      if (i == 1 && mp == 3) continue;
      const int ftile = i ? ft1 : ft0;
      const u32x2 zz = zz1[i][n];
      float g[4];
#pragma unroll
      for (int j = 0; j < 4; ++j) g[j] = rbf(rbf(acc[i][n][j]) + bb);
      u32x2 o = {pack2(lo2f(zz[0]) * g[0], hi2f(zz[0]) * g[1]), pack2(lo2f(zz[1]) * g[2], hi2f(zz[1]) * g[3])};
      const int off = ((((tb * 4 + 2 * kh + n) * 28 + (ftile >> 1)) * 64 + 16 * (2 * (ftile & 1) + (q >> 1)) + r) * 8 + 4 * (q & 1)) * 2;
      __builtin_amdgcn_raw_buffer_store_b64(o, urs, off, 0, 16);
    }
  }
  asm volatile("s_waitcnt vmcnt(0)" ::: "memory");
  __syncthreads();
  if (tid == 0) __hip_atomic_fetch_add(cnt + tb * 32, 1u, __ATOMIC_RELAXED, __HIP_MEMORY_SCOPE_AGENT);
  STAMP(2, 5);

  u32x2 hh[2];
#pragma unroll
  for (int n = 0; n < 2; ++n)
    hh[n] = *(const u32x2*)(hin + (size_t)(T0 + 32 * th + 16 * n + r) * D_ + dq * 112 + 16 * wv + 4 * q);
  if (tid == 0) {
    for (int spin = 0; spin < (1 << 22); ++spin) {
      const unsigned c = __hip_atomic_load(cnt + tb * 32, __ATOMIC_RELAXED, __HIP_MEMORY_SCOPE_AGENT);
      if (c >= 8u) break;
      __builtin_amdgcn_s_sleep(1);
    }
  }
  __syncthreads();
  STAMP(2, 6);
  {
    u32x4 ul[7];
#pragma unroll
    for (int j = 0; j < 7; ++j)
      ul[j] = __builtin_amdgcn_raw_buffer_load_b128(urs, (tb * 64 + th * 32) * FH_ * 2 + (tid + 512 * j) * 16, 0, 16);
#pragma unroll
    for (int j = 0; j < 7; ++j) *(u32x4*)(smem + (tid + 512 * j) * 16) = ul[j];
  }
  __syncthreads();
  STAMP(2, 7);
  if (wave < 7) {
    f32x4 ac2[2];
#pragma unroll
    for (int n = 0; n < 2; ++n) ac2[n] = (f32x4){0.f, 0.f, 0.f, 0.f};
#pragma unroll
    for (int ks = 0; ks < 28; ++ks) {
#pragma unroll
      for (int n = 0; n < 2; ++n) {
        const bf16x8 b = *(const bf16x8*)(smem + ((n * 28 + ks) * 64 + lane) * 16);
        ac2[n] = mfma16(wf[ks], b, ac2[n]);
      }
    }
#pragma unroll
    for (int n = 0; n < 2; ++n) {
      u32x2 o = {pack2(lo2f(hh[n][0]) + rbf(ac2[n][0]), hi2f(hh[n][0]) + rbf(ac2[n][1])),
                 pack2(lo2f(hh[n][1]) + rbf(ac2[n][2]), hi2f(hh[n][1]) + rbf(ac2[n][3]))};
      *(u32x2*)(out + (size_t)(T0 + 32 * th + 16 * n + r) * D_ + dq * 112 + 16 * wave + 4 * q) = o;
    }
  }
  STAMP(2, 8);
}

extern "C" void kernel_launch(void* const* d_in, const int* in_sizes, int n_in, void* d_out,
                              int out_size, void* d_ws, size_t ws_size, hipStream_t stream) {
  const u16* xq = (const u16*)d_in[0];
  const u16* kin = (const u16*)d_in[1];
  const u16* vin = (const u16*)d_in[2];
  const u16* shk = (const u16*)d_in[3];
  const u16* ssmk = (const u16*)d_in[4];
  const u16* ssmD = (const u16*)d_in[5];
  const u16* w_in = (const u16*)d_in[7];
  const u16* w_sp = (const u16*)d_in[8];
  const u16* b_sp = (const u16*)d_in[9];
  const u16* w_out = (const u16*)d_in[10];
  char* ws = (char*)d_ws;
  u16* yT = (u16*)(ws + WS_YT);
  u16* h = (u16*)(ws + WS_H);
  u16* w_inT = (u16*)(ws + WS_WINT);
  u16* w_outT = (u16*)(ws + WS_WOUTT);
  float2* ln1 = (float2*)(ws + WS_LN1);
  u16* z1 = (u16*)(ws + WS_Z1);
  u16* z2T = (u16*)(ws + WS_Z2T);
  unsigned long long* pst = (unsigned long long*)(ws + WS_PST);
  u16* kvr = (u16*)(ws + WS_KVR);
  u16* u = (u16*)(ws + WS_U);

  k0_prep<<<256, 512, 0, stream>>>(kin, vin, shk, kvr, xq, w_in, w_out, w_inT, w_outT, ln1, pst, ssmk);
  k1_conv<<<224, 512, 0, stream>>>(kvr, ssmk, ssmD, yT, w_sp);
  k2_gemm1<<<256, 512, 0, stream>>>(xq, yT, ln1, w_inT, h, z1, z2T, pst);
  k3_gemm2<<<256, 512, 0, stream>>>(w_sp, b_sp, z2T, z1, u, w_outT, h, (u16*)d_out, (unsigned*)(pst + 32 * 512));
}
```

```cpp
#include <hip/hip_runtime.h>
#include <hip/hip_bf16.h>
#include <stdint.h>

typedef __attribute__((ext_vector_type(8))) short bf16x8;
typedef __attribute__((ext_vector_type(4))) short s16x4;
typedef __attribute__((ext_vector_type(4))) float f32x4;
typedef __attribute__((ext_vector_type(2))) float f32x2;
typedef __attribute__((ext_vector_type(4))) unsigned int u32x4;
typedef __attribute__((ext_vector_type(2))) unsigned int u32x2;
typedef __attribute__((ext_vector_type(2))) __bf16 bf16x2_t;
typedef unsigned short u16;

#define DEVI __device__ __forceinline__
#define STAMP(kid, i)

constexpr int S_ = 2048, D_ = 448, F_ = 1792, FH_ = 896;

DEVI float lo2f(unsigned v) { return __uint_as_float(v << 16); }
DEVI float hi2f(unsigned v) { return __uint_as_float(v & 0xffff0000u); }
DEVI float bf2f(u16 v) { return __uint_as_float(((unsigned)v) << 16); }
DEVI unsigned pack2(float a, float b) {
  f32x2 f = {a, b};
  bf16x2_t h = __builtin_convertvector(f, bf16x2_t);
  return __builtin_bit_cast(unsigned, h);
}
DEVI float rbf(float x) { return lo2f(pack2(x, x)); }
DEVI f32x4 mfma16(bf16x8 a, bf16x8 b, f32x4 c) {
  return __builtin_amdgcn_mfma_f32_16x16x32_bf16(a, b, c, 0, 0, 0);
}
template <int CTRL> DEVI float dpp_f(float x) {
  return __builtin_bit_cast(float, __builtin_amdgcn_update_dpp(0, __builtin_bit_cast(int, x), CTRL, 0xf, 0xf, false));
}
DEVI float row16_sum(float x) {
  x += dpp_f<0x121>(x);
  x += dpp_f<0x122>(x);
  x += dpp_f<0x124>(x);
  x += dpp_f<0x128>(x);
  return x;
}
DEVI float gelu_tanh(float x) {
  const float u = x * (0.7978845608028654f + 0.035677408136300125f * x * x);
  const float e = __builtin_amdgcn_exp2f(-2.885390081777927f * u);
  return x * __builtin_amdgcn_rcpf(1.0f + e);
}

constexpr size_t WS_YT = 0;
constexpr size_t WS_H = WS_YT + (size_t)D_ * S_ * 2;
constexpr size_t WS_WINT = WS_H + (size_t)S_ * D_ * 2;
constexpr size_t WS_WOUTT = WS_WINT + (size_t)F_ * D_ * 2;
constexpr size_t WS_LN1 = WS_WOUTT + (size_t)D_ * FH_ * 2;
constexpr size_t WS_Z1 = WS_LN1 + (size_t)S_ * 8;
constexpr size_t WS_Z2T = WS_Z1 + (size_t)S_ * FH_ * 2;
constexpr size_t WS_PST = WS_Z2T + (size_t)S_ * FH_ * 2;
constexpr size_t WS_U = WS_PST + (size_t)9 * S_ * 8;
constexpr size_t WS_KVR = WS_U + (size_t)S_ * FH_ * 2;

constexpr int K1_XREV = 0;
constexpr int K1_XSTRIDE = 2304 * 2;
constexpr int K1_COP = 9216;
constexpr int K1_CSTRIDE = 4128;
constexpr int K1_RED = K1_COP + 2 * 8 * K1_CSTRIDE;
constexpr int K1_LDS = K1_RED + 8 * 8192;

DEVI float wave_sum(float x) {
  x = row16_sum(x);
  const int xi = __builtin_bit_cast(int, x);
  return __builtin_bit_cast(float, __builtin_amdgcn_readlane(xi, 0)) + __builtin_bit_cast(float, __builtin_amdgcn_readlane(xi, 16)) +
         __builtin_bit_cast(float, __builtin_amdgcn_readlane(xi, 32)) + __builtin_bit_cast(float, __builtin_amdgcn_readlane(xi, 48));
}

__global__ __launch_bounds__(512) void k0_prep(
    const u16* __restrict__ kin, const u16* __restrict__ vin, const u16* __restrict__ shk,
    u16* __restrict__ kvr,
    const u16* __restrict__ xin, const u16* __restrict__ w_in, const u16* __restrict__ w_out,
    u16* __restrict__ w_inT, u16* __restrict__ w_outT, float2* __restrict__ ln1,
    unsigned long long* __restrict__ gran, const u16* __restrict__ ssmk, const char* __restrict__ wsbase, const u16* __restrict__ w_sp) {
  __shared__ __attribute__((aligned(16))) char smem[8 * 4608 + 64 * 144];
  const int tid = threadIdx.x, lane = tid & 63;
  const int wave = __builtin_amdgcn_readfirstlane(tid >> 6);
  const int blk = blockIdx.x;
  STAMP(4, 0);
  const int gw = blk * 8 + wave;
  const int r = lane & 15, q = lane >> 4;

  const bool has_tile = blk < 224;
  const int S0 = (blk / 7) * 64, D0 = (blk % 7) * 64;
  const int row = tid >> 3, c8 = (tid & 7) * 8;
  u32x4 kq[4], vq = {0u, 0u, 0u, 0u}, sk[4];
  if (has_tile) {
    const int s = S0 + row;
#pragma unroll
    for (int j = 0; j < 4; ++j) {
      kq[j] = (u32x4){0u, 0u, 0u, 0u};
      if (s - 3 + j >= 0) kq[j] = *(const u32x4*)(kin + (size_t)(s - 3 + j) * D_ + D0 + c8);
    }
    vq = *(const u32x4*)(vin + (size_t)s * D_ + D0 + c8);
#pragma unroll
    for (int j = 0; j < 4; ++j) sk[j] = *(const u32x4*)(shk + (D0 + c8) * 4 + j * 8);
  }
  u32x4 xr = {0u, 0u, 0u, 0u};
  if (lane < 56) xr = *(const u32x4*)(xin + (size_t)gw * D_ + lane * 8);
  const bool has_slab = gw < 588;
  u32x4 sl[4];
  if (has_slab) {
    const u16* src; int ld;
    if (gw < 392) { src = w_in + (size_t)(gw / 28) * 32 * F_ + (gw % 28) * 64; ld = F_; }
    else { const int i2 = gw - 392; src = w_out + (size_t)(i2 / 7) * 32 * D_ + (i2 % 7) * 64; ld = D_; }
#pragma unroll
    for (int i = 0; i < 4; ++i)
      sl[i] = *(const u32x4*)(src + (size_t)(8 * i + (lane >> 3)) * ld + (lane & 7) * 8);
  }
  const u32x4 pf = ((const u32x4*)ssmk)[blk < 224 ? blk * 512 + tid : tid];
  unsigned pt = 0;
  if (wave == 7) pt = *(const unsigned*)(wsbase + (size_t)lane * 458752) + *(const unsigned*)((const char*)w_sp + (size_t)lane * 131072);
  if (blk < 33) gran[blk * 512 + tid] = 0ull;

  STAMP(4, 1);
  u16* tl = (u16*)(smem + 8 * 4608);
  if (has_tile) {
#pragma unroll
    for (int e = 0; e < 8; ++e) {
      const unsigned s01 = sk[e >> 1][(e & 1) * 2], s23 = sk[e >> 1][(e & 1) * 2 + 1];
      float kf[4];
#pragma unroll
      for (int j = 0; j < 4; ++j) kf[j] = (e & 1) ? hi2f(kq[j][e >> 1]) : lo2f(kq[j][e >> 1]);
      float a = rbf(kf[0] * hi2f(s23));
      a = rbf(a + rbf(kf[1] * lo2f(s23)));
      a = rbf(a + rbf(kf[2] * hi2f(s01)));
      a = rbf(a + rbf(kf[3] * lo2f(s01)));
      const float vf = (e & 1) ? hi2f(vq[e >> 1]) : lo2f(vq[e >> 1]);
      tl[(c8 + e) * 72 + (63 - row)] = (u16)(pack2(a * vf, 0.f) & 0xffffu);
    }
  }
  STAMP(4, 2);
  {
    float v[8];
#pragma unroll
    for (int j = 0; j < 4; ++j) { v[2 * j] = lo2f(xr[j]); v[2 * j + 1] = hi2f(xr[j]); }
    float sm = 0.f;
#pragma unroll
    for (int j = 0; j < 8; ++j) sm += v[j];
    const float mean = wave_sum(sm) * (1.0f / D_);
    float m2 = 0.f;
    if (lane < 56) {
#pragma unroll
      for (int j = 0; j < 8; ++j) { float d = v[j] - mean; m2 += d * d; }
    }
    m2 = wave_sum(m2);
    if (lane == 0) {
      const float var = rbf(m2 * (1.0f / D_));
      const float sd = rbf(sqrtf(rbf(var + 1e-5f)));
      ln1[gw] = make_float2(rbf(mean), 1.0f / sd);
    }
  }
  STAMP(4, 3);
  if (has_slab) {
    typedef __attribute__((address_space(3))) s16x4* lp;
    char* ts = smem + wave * 4608;
    u16* dst; int bstride;
    if (gw < 392) { dst = w_inT + (size_t)((gw % 28) * 4 * 14 + (gw / 28)) * 512; bstride = 14 * 512; }
    else { const int i2 = gw - 392; dst = w_outT + (size_t)((i2 % 7) * 4 * 28 + (i2 / 7)) * 512; bstride = 28 * 512; }
#pragma unroll
    for (int i = 0; i < 4; ++i) *(u32x4*)(ts + (8 * i + (lane >> 3)) * 144 + (lane & 7) * 16) = sl[i];
#pragma unroll
    for (int n = 0; n < 4; ++n) {
      const char* a0p = ts + (8 * q + (r >> 2)) * 144 + (16 * n + 4 * (r & 3)) * 2;
      s16x4 v0 = __builtin_amdgcn_ds_read_tr16_b64_v4i16((lp)(a0p));
      s16x4 v1 = __builtin_amdgcn_ds_read_tr16_b64_v4i16((lp)(a0p + 4 * 144));
      bf16x8 o = {v0[0], v0[1], v0[2], v0[3], v1[0], v1[1], v1[2], v1[3]};
      *(bf16x8*)(dst + (size_t)n * bstride + lane * 8) = o;
    }
  }
  STAMP(4, 4);
  if (has_tile) {
    __syncthreads();
    const u32x4 o = *(const u32x4*)(tl + row * 72 + c8);
    *(u32x4*)(kvr + (size_t)(D0 + row) * S_ + (1984 - S0) + c8) = o;
  }
  asm volatile("" ::"v"(pf), "v"(pt));
  STAMP(4, 5);
}

__global__ __launch_bounds__(512) void k1_conv(
    const u16* __restrict__ kvr, const u16* __restrict__ ssmk, const u16* __restrict__ ssmD,
    u16* __restrict__ yT, const u16* __restrict__ w_sp) {
  __shared__ __attribute__((aligned(16))) char smem[K1_LDS];
  const int tid = threadIdx.x, lane = tid & 63, wave = tid >> 6;
  STAMP(0, 0);
  const int xcd = blockIdx.x & 7, idx = blockIdx.x >> 3;
  const int d0 = (xcd * 28 + idx) * 2;
  const int tch = tid >> 8, mm = tid & 255;
  const u16* krow = ssmk + (size_t)(d0 + tch) * S_;
  const u32x4 xr = *(const u32x4*)(kvr + (size_t)(d0 + tch) * S_ + mm * 8);
  u32x4 wa = *(const u32x4*)(krow + mm * 8);
  u32x4 wb = {0u, 0u, 0u, 0u};
  if (mm < 255) wb = *(const u32x4*)(krow + mm * 8 + 8);
  const unsigned dDw = *(const unsigned*)(ssmD + d0);
  *(u32x4*)(smem + K1_XREV + tch * K1_XSTRIDE + mm * 16) = xr;

  if (tid < 64) {
    u32x4 z = {0u, 0u, 0u, 0u};
    *(u32x4*)(smem + K1_XREV + (tid >> 5) * K1_XSTRIDE + (2048 + (tid & 31) * 8) * 2) = z;
  }
  {
    unsigned w[8] = {wa[0], wa[1], wa[2], wa[3], wb[0], wb[1], wb[2], wb[3]};
    unsigned al[7];
#pragma unroll
    for (int i = 0; i < 7; ++i) al[i] = (w[i] >> 16) | (w[i + 1] << 16);
    char* base = smem + K1_COP + tch * 8 * K1_CSTRIDE + (mm + 2) * 16;
#pragma unroll
    for (int c = 0; c < 8; ++c) {
      u32x4 o;
      if ((c & 1) == 0) { o[0] = w[c / 2]; o[1] = w[c / 2 + 1]; o[2] = w[c / 2 + 2]; o[3] = w[c / 2 + 3]; }
      else { o[0] = al[c / 2]; o[1] = al[c / 2 + 1]; o[2] = al[c / 2 + 2]; o[3] = al[c / 2 + 3]; }
      *(u32x4*)(base + c * K1_CSTRIDE) = o;
    }
    if (mm < 2) {
      unsigned w2[8] = {0u, 0u, 0u, 0u, 0u, 0u, 0u, 0u};
      if (mm == 0) { w2[4] = wa[0]; w2[5] = wa[1]; w2[6] = wa[2]; w2[7] = wa[3]; }
      unsigned a2[7];
#pragma unroll
      for (int i = 0; i < 7; ++i) a2[i] = (w2[i] >> 16) | (w2[i + 1] << 16);
      char* b2 = smem + K1_COP + tch * 8 * K1_CSTRIDE + (1 - mm) * 16;
#pragma unroll
      for (int c = 0; c < 8; ++c) {
        u32x4 o;
        if ((c & 1) == 0) { o[0] = w2[c / 2]; o[1] = w2[c / 2 + 1]; o[2] = w2[c / 2 + 2]; o[3] = w2[c / 2 + 3]; }
        else { o[0] = a2[c / 2]; o[1] = a2[c / 2 + 1]; o[2] = a2[c / 2 + 2]; o[3] = a2[c / 2 + 3]; }
        *(u32x4*)(b2 + c * K1_CSTRIDE) = o;
      }
    }
  }
  STAMP(0, 1);
  __syncthreads();
  STAMP(0, 2);

  u32x4 pf[5];
  {
    const int base = (xcd * 28 + idx) * 2560 + tid;
#pragma unroll
    for (int j = 0; j < 5; ++j) {
      const int c = base + j * 512;
      pf[j] = ((const u32x4*)w_sp)[c < S_ * S_ / 8 ? c : 0];
    }
  }
  {
    const int ch = wave & 1, r = lane & 15, q = lane >> 4;
    f32x4 acc[8];
#pragma unroll
    for (int t = 0; t < 8; ++t) acc[t] = (f32x4){0.f, 0.f, 0.f, 0.f};
    const char* abase = smem + K1_COP + ch * 8 * K1_CSTRIDE + ((r + 1) & 7) * K1_CSTRIDE +
                        (2 * (q >> 1) - (q & 1) + ((r + 9) >> 3)) * 16;
    const char* bbase = smem + K1_XREV + ch * K1_XSTRIDE + (2040 - 16 * r + 16 * (q >> 1) - 8 * (q & 1)) * 2;
#pragma unroll
    for (int cc = 0; cc < 2; ++cc) {
      const int rho = 2 * (wave >> 1) + cc;
      bf16x8 A[8], B[8];
#pragma unroll
      for (int i = 0; i < 8; ++i) A[i] = *(const bf16x8*)(abase + (rho + 8 * i) * 64);
#pragma unroll
      for (int i = 0; i < 8; ++i) B[i] = *(const bf16x8*)(bbase - (8 * i - rho) * 64);
#pragma unroll
      for (int t = 0; t < 8; ++t)
#pragma unroll
        for (int i = 0; i <= t; ++i) acc[t] = mfma16(A[i], B[t - i], acc[t]);
    }
    float* red = (float*)(smem + K1_RED + wave * 8192);
#pragma unroll
    for (int t = 0; t < 8; ++t) *(f32x4*)(red + 256 * t + 16 * r + 4 * q) = acc[t];
  }
  STAMP(0, 3);
  __syncthreads();
  STAMP(0, 4);
  {
    const int ch = tid >> 8, e0 = (tid & 255) * 8;
    f32x4 sa = {0.f, 0.f, 0.f, 0.f}, sb = {0.f, 0.f, 0.f, 0.f};
#pragma unroll
    for (int w = 0; w < 4; ++w) {
      const float* red = (const float*)(smem + K1_RED + (2 * w + ch) * 8192);
      sa += *(const f32x4*)(red + e0);
      sb += *(const f32x4*)(red + e0 + 4);
    }
    const u32x4 kr = *(const u32x4*)(smem + K1_XREV + ch * K1_XSTRIDE + (2040 - e0) * 2);
    const float Dv = ch ? hi2f(dDw) : lo2f(dDw);
    float kvf[8];
    kvf[7] = lo2f(kr[0]); kvf[6] = hi2f(kr[0]); kvf[5] = lo2f(kr[1]); kvf[4] = hi2f(kr[1]);
    kvf[3] = lo2f(kr[2]); kvf[2] = hi2f(kr[2]); kvf[1] = lo2f(kr[3]); kvf[0] = hi2f(kr[3]);
    float y[8];
#pragma unroll
    for (int j = 0; j < 4; ++j) {
      y[j] = rbf(sa[j]) + rbf(kvf[j] * Dv);
      y[j + 4] = rbf(sb[j]) + rbf(kvf[j + 4] * Dv);
    }
    u32x4 o = {pack2(y[0], y[1]), pack2(y[2], y[3]), pack2(y[4], y[5]), pack2(y[6], y[7])};
    *(u32x4*)(yT + (size_t)(d0 + ch) * S_ + e0) = o;
  }
#pragma unroll
  for (int j = 0; j < 5; ++j) asm volatile("" ::"v"(pf[j]));
  STAMP(0, 5);
}

constexpr int K2_YT = 0;
constexpr int K2_AT = 448 * 80;
constexpr int K2_ST = K2_AT + 32 * 1024;
constexpr int K2_FS = K2_ST + 8 * 32 * 8;
constexpr int K2_LDS = K2_FS + 32 * 8;

DEVI f32x2 unp(unsigned v) { return (f32x2){lo2f(v), hi2f(v)}; }
DEVI unsigned pk(f32x2 v) { return pack2(v[0], v[1]); }

__global__ __launch_bounds__(512) void k2_gemm1(
    const u16* __restrict__ xin, const u16* __restrict__ yT, const float2* __restrict__ ln1,
    const u16* __restrict__ w_inT, u16* __restrict__ hout, u16* __restrict__ z1,
    u16* __restrict__ z2T, unsigned long long* pstat) {
  __shared__ __attribute__((aligned(16))) char smem[K2_LDS];
  const int tid = threadIdx.x, lane = tid & 63;
  const int wave = __builtin_amdgcn_readfirstlane(tid >> 6);
  STAMP(1, 0);
  const int xcd = blockIdx.x & 7, idx = blockIdx.x >> 3;
  const int rb = xcd * 8 + (idx & 7), cb = idx >> 3;
  const int R0 = rb * 32;
  const int r = lane & 15, q = lane >> 4;

  u32x4 yl[4], xg[4];
#pragma unroll
  for (int j = 0; j < 4; ++j) {
    const int id = tid + 512 * j;
    if (j < 3 || tid < 256) {
      yl[j] = *(const u32x4*)(yT + (size_t)(id >> 2) * S_ + R0 + (id & 3) * 8);
      xg[j] = *(const u32x4*)(xin + (size_t)R0 * D_ + id * 8);
    }
  }
  const int prow = 16 * (wave & 1) + r;
  const int dq0 = (wave >> 1) * 28 + q;
  const float2 l1 = ln1[R0 + prow];
  const int wv = wave < 7 ? wave : 6;
  const u16* wz2 = w_inT + (size_t)((56 + cb * 14 + 2 * wv) * 14 * 64 + lane) * 8;
  const u16* wz1 = w_inT + (size_t)((cb * 14 + 2 * wv) * 14 * 64 + lane) * 8;
  bf16x8 wf[2][14];
#pragma unroll
  for (int ks = 0; ks < 7; ++ks)
#pragma unroll
    for (int i = 0; i < 2; ++i) wf[i][ks] = *(const bf16x8*)(wz2 + (i * 14 + ks) * 512);
#pragma unroll
  for (int j = 0; j < 4; ++j) {
    const int id = tid + 512 * j;
    if (j < 3 || tid < 256) {
      *(u32x4*)(smem + K2_YT + (id >> 2) * 80 + (id & 3) * 16) = yl[j];
      const int row = id / 56, c = id % 56;
      *(u32x4*)(smem + K2_AT + row * 1024 + ((c ^ (row & 15)) * 16)) = xg[j];
    }
  }
  STAMP(1, 1);
  __syncthreads();
  STAMP(1, 2);
  f32x2 hv[7][2];
  f32x2 sum2 = {0.f, 0.f};
  {
    typedef __attribute__((address_space(3))) s16x4* lp;
    const int qq = r >> 2, pp = r & 3;
    const f32x2 m1 = {l1.x, l1.x}, r1 = {l1.y, l1.y};
#pragma unroll
    for (int it = 0; it < 7; ++it) {
      const int dq = dq0 + 4 * it;
      const char* a0 = smem + K2_YT + (4 * dq + qq) * 80 + (16 * (wave & 1) + 4 * pp) * 2;
      const u32x2 y0 = __builtin_bit_cast(u32x2, __builtin_amdgcn_ds_read_tr16_b64_v4i16((lp)(a0)));
      const u32x2 xv = *(const u32x2*)(smem + K2_AT + prow * 1024 + (((dq >> 1) ^ r) * 16) + (dq & 1) * 8);
#pragma unroll
      for (int j = 0; j < 2; ++j) {
        const f32x2 x2 = unp(xv[j]);
        const unsigned dd = pk(x2 - m1);
        const unsigned qn = pk(unp(dd) * r1);
        const unsigned aa = pk(unp(qn) * unp(y0[j]));
        hv[it][j] = unp(pk(x2 + unp(aa)));
        sum2 += hv[it][j];
      }
#pragma unroll
      for (int i = 0; i < 2; ++i) wf[i][7 + it] = *(const bf16x8*)(wz2 + (i * 14 + 7 + it) * 512);
      __builtin_amdgcn_sched_barrier(0);
    }
  }
  float sum = sum2[0] + sum2[1];
  sum += __shfl_xor(sum, 16);
  sum += __shfl_xor(sum, 32);
  const float mh = sum * (1.0f / 112.0f);
  f32x2 q2 = {0.f, 0.f};
  {
    const f32x2 mh2 = {mh, mh};
#pragma unroll
    for (int it = 0; it < 7; ++it)
#pragma unroll
      for (int j = 0; j < 2; ++j) {
        const f32x2 d = hv[it][j] - mh2;
        q2 += d * d;
      }
  }
  float m2 = q2[0] + q2[1];
  m2 += __shfl_xor(m2, 16);
  m2 += __shfl_xor(m2, 32);
  float2* st = (float2*)(smem + K2_ST);
  if (q == 0) st[wave * 16 + r] = make_float2(mh, m2);
  STAMP(1, 3);
  __syncthreads();
  STAMP(1, 4);
  {
    float2 pq[4];
    float ms = 0.f;
#pragma unroll
    for (int w = 0; w < 4; ++w) { pq[w] = st[((wave & 1) + 2 * w) * 16 + r]; ms += pq[w].x; }
    const float mean = 0.25f * ms;
    float M2 = 0.f;
#pragma unroll
    for (int w = 0; w < 4; ++w) { const float dl = pq[w].x - mean; M2 += pq[w].y + 112.0f * dl * dl; }
    const float var = rbf(M2 * (1.0f / D_));
    const float mb = rbf(mean);
    const float rinv = 1.0f / rbf(sqrtf(rbf(var + 1e-5f)));
    const f32x2 mb2 = {mb, mb}, ri2 = {rinv, rinv};
#pragma unroll
    for (int it = 0; it < 7; ++it) {
      u32x2 o2;
#pragma unroll
      for (int j = 0; j < 2; ++j) o2[j] = pk(unp(pk(hv[it][j] - mb2)) * ri2);
      const int dq = dq0 + 4 * it;
      *(u32x2*)(smem + K2_AT + prow * 1024 + (((dq >> 1) ^ r) * 16) + (dq & 1) * 8) = o2;
      if (cb == 0) {
        u32x2 h2 = {pk(hv[it][0]), pk(hv[it][1])};
        *(u32x2*)(hout + (size_t)(R0 + prow) * D_ + dq * 4) = h2;
      }
    }
  }
  STAMP(1, 5);
  __syncthreads();
  STAMP(1, 6);

  u32x2 zp[2][2];
  bf16x8 wg[2][14];
  if (wave < 7) {
    f32x4 acc[2][2];
#pragma unroll
    for (int i = 0; i < 2; ++i)
#pragma unroll
      for (int s = 0; s < 2; ++s) acc[i][s] = (f32x4){0.f, 0.f, 0.f, 0.f};
#pragma unroll
    for (int ks = 0; ks < 14; ++ks) {
      bf16x8 xf[2];
#pragma unroll
      for (int s = 0; s < 2; ++s)
        xf[s] = *(const bf16x8*)(smem + K2_AT + (16 * s + r) * 1024 + (((4 * ks + q) ^ r) * 16));
#pragma unroll
      for (int i = 0; i < 2; ++i)
#pragma unroll
        for (int s = 0; s < 2; ++s) acc[i][s] = mfma16(xf[s], wf[i][ks], acc[i][s]);
#pragma unroll
      for (int i = 0; i < 2; ++i) wg[i][ks] = *(const bf16x8*)(wz1 + (i * 14 + ks) * 512);
    }
#pragma unroll
    for (int s = 0; s < 2; ++s) {
#pragma unroll
      for (int i = 0; i < 2; ++i) {
        float zz[4];
#pragma unroll
        for (int j = 0; j < 4; ++j) zz[j] = gelu_tanh(rbf(acc[i][s][j]));
        zp[i][s] = (u32x2){pack2(zz[0], zz[1]), pack2(zz[2], zz[3])};
      }
      float mw[4], qw[4];
#pragma unroll
      for (int j = 0; j < 4; ++j) {
        const float v0 = (j & 1) ? hi2f(zp[0][s][j >> 1]) : lo2f(zp[0][s][j >> 1]);
        const float v1 = (j & 1) ? hi2f(zp[1][s][j >> 1]) : lo2f(zp[1][s][j >> 1]);
        mw[j] = row16_sum(v0 + v1) * (1.0f / 32.0f);
        const float a0 = v0 - mw[j], a1 = v1 - mw[j];
        qw[j] = row16_sum(a0 * a0 + a1 * a1);
      }
      if (r == 0) {
#pragma unroll
        for (int j = 0; j < 4; ++j) st[wave * 32 + 16 * s + 4 * q + j] = make_float2(mw[j], qw[j]);
      }
    }
  }
  __syncthreads();
  if (wave == 7) {
    float2* fs = (float2*)(smem + K2_FS);
    const int row = lane >> 1;
    float msum = 0.f;
    float2 pw[7];
#pragma unroll
    for (int w = 0; w < 7; ++w) { pw[w] = st[w * 32 + row]; msum += pw[w].x; }
    const float bmean = msum * (1.0f / 7.0f);
    float bM2 = 0.f;
#pragma unroll
    for (int w = 0; w < 7; ++w) { const float dl = pw[w].x - bmean; bM2 += pw[w].y + 32.0f * dl * dl; }
    const float mine = (lane & 1) ? bM2 : bmean;
    unsigned long long* g = pstat + (size_t)rb * 256 + lane;
    __hip_atomic_store(g + cb * 64, (1ull << 32) | __float_as_uint(mine), __ATOMIC_RELAXED, __HIP_MEMORY_SCOPE_AGENT);
    float pv[4];
    for (int spin = 0; spin < (1 << 22); ++spin) {
      bool ok = true;
#pragma unroll
      for (int c = 0; c < 4; ++c) {
        const unsigned long long v = __hip_atomic_load(g + c * 64, __ATOMIC_RELAXED, __HIP_MEMORY_SCOPE_AGENT);
        pv[c] = __uint_as_float((unsigned)v);
        ok &= (v >> 32) == 1ull;
      }
      if (__all(ok)) break;
      __builtin_amdgcn_s_sleep(1);
    }
    float pm[4], pq[4];
#pragma unroll
    for (int c = 0; c < 4; ++c) {
      const float o = __shfl_xor(pv[c], 1);
      pm[c] = (lane & 1) ? o : pv[c];
      pq[c] = (lane & 1) ? pv[c] : o;
    }
    const float mean = (pm[0] + pm[1] + pm[2] + pm[3]) * 0.25f;
    float M2 = 0.f;
#pragma unroll
    for (int c = 0; c < 4; ++c) { const float dl = pm[c] - mean; M2 += pq[c] + 224.0f * dl * dl; }
    const float var = rbf(M2 * (1.0f / FH_));
    if ((lane & 1) == 0) fs[row] = make_float2(rbf(mean), 1.0f / rbf(sqrtf(rbf(var + 1e-5f))));
  } else {
    STAMP(1, 7);
    f32x4 acc[2][2];
#pragma unroll
    for (int i = 0; i < 2; ++i)
#pragma unroll
      for (int s = 0; s < 2; ++s) acc[i][s] = (f32x4){0.f, 0.f, 0.f, 0.f};
#pragma unroll
    for (int ks = 0; ks < 14; ++ks) {
      bf16x8 xf[2];
#pragma unroll
      for (int s = 0; s < 2; ++s)
        xf[s] = *(const bf16x8*)(smem + K2_AT + (16 * s + r) * 1024 + (((4 * ks + q) ^ r) * 16));
#pragma unroll
      for (int i = 0; i < 2; ++i)
#pragma unroll
        for (int s = 0; s < 2; ++s) acc[i][s] = mfma16(wg[i][ks], xf[s], acc[i][s]);
    }
#pragma unroll
    for (int s = 0; s < 2; ++s)
#pragma unroll
      for (int i = 0; i < 2; ++i) {
        float zz[4];
#pragma unroll
        for (int j = 0; j < 4; ++j) zz[j] = gelu_tanh(rbf(acc[i][s][j]));
        u32x2 o = {pack2(zz[0], zz[1]), pack2(zz[2], zz[3])};
        *(u32x2*)(z1 + (size_t)(((rb * 2 + s) * 56 + cb * 14 + 2 * wave + i) * 64 + lane) * 4) = o;
      }
    STAMP(1, 8);
  }
  __syncthreads();
  STAMP(1, 9);
  if (wave < 7) {
    const float2* fs = (const float2*)(smem + K2_FS);
#pragma unroll
    for (int s = 0; s < 2; ++s) {
      const f32x4 sa = *(const f32x4*)(fs + 16 * s + 4 * q), sb = *(const f32x4*)(fs + 16 * s + 4 * q + 2);
#pragma unroll
      for (int i = 0; i < 2; ++i) {
        const float n0 = rbf(lo2f(zp[i][s][0]) - sa[0]) * sa[1], n1 = rbf(hi2f(zp[i][s][0]) - sa[2]) * sa[3];
        const float n2 = rbf(lo2f(zp[i][s][1]) - sb[0]) * sb[1], n3 = rbf(hi2f(zp[i][s][1]) - sb[2]) * sb[3];
        u32x2 o = {pack2(n0, n1), pack2(n2, n3)};
        const int ftile = cb * 14 + 2 * wave + i;
        const int lq = 2 * s + (q >> 1);
        *(u32x2*)(z2T + (size_t)((ftile * 64 + rb) * 64 + 16 * lq + r) * 8 + 4 * (q & 1)) = o;
      }
    }
  }
  STAMP(1, 10);
}

constexpr int K3_WB = 0;
constexpr int K3_LDS = 32 * 1792;
constexpr int K3_D = 4;

__global__ __launch_bounds__(512) void k3_gemm2(
    const u16* __restrict__ w_sp, const u16* __restrict__ b_sp, const u16* __restrict__ z2T,
    const u16* __restrict__ z1, u16* uout, const u16* __restrict__ w_outT,
    const u16* __restrict__ hin, u16* __restrict__ out, unsigned* cnt) {
  __shared__ __attribute__((aligned(16))) char smem[K3_LDS];
  const int tid = threadIdx.x, lane = tid & 63;
  const int wave = __builtin_amdgcn_readfirstlane(tid >> 6);
  STAMP(2, 0);
  const int xcd = blockIdx.x & 7, idx = blockIdx.x >> 3;
  const int tb = (xcd & 3) * 8 + (idx & 7), fb = (xcd >> 2) * 4 + (idx >> 3);
  const int T0 = tb * 64;
  const int r = lane & 15, q = lane >> 4;
  const int kh = wave >> 2, mp = wave & 3;
  constexpr int NT = 16;

  const u16* wsrc = w_sp + (size_t)(T0 + (tid >> 4)) * S_ + (tid & 15) * 8;
  const int ft0 = fb * 7 + 2 * mp, ft1 = fb * 7 + (mp < 3 ? 2 * mp + 1 : 2 * mp);
  const u16* zsrc0 = z2T + (size_t)((ft0 * 64 + kh) * 64 + lane) * 8;
  const u16* zsrc1 = z2T + (size_t)((ft1 * 64 + kh) * 64 + lane) * 8;
  const int wdst = (tid >> 4) * 256 + (((tid & 15) ^ ((tid >> 4) & 15)) * 16);

  u32x4 wreg[K3_D][2];
  bf16x8 zreg[K3_D][2][2];
  auto ldtile = [&](int d) {
#pragma unroll
    for (int j = 0; j < 2; ++j) wreg[d][j] = *(const u32x4*)(wsrc + (size_t)(32 * j) * S_ + d * 128);
#pragma unroll
    for (int k2 = 0; k2 < 2; ++k2) {
      zreg[d][0][k2] = *(const bf16x8*)(zsrc0 + (d * 4 + 2 * k2) * 512);
      zreg[d][1][k2] = *(const bf16x8*)(zsrc1 + (d * 4 + 2 * k2) * 512);
    }
  };
  ldtile(0);
  ldtile(1);
  auto stageW = [&](int it) {
    const int d = it % K3_D;
#pragma unroll
    for (int j = 0; j < 2; ++j) *(u32x4*)(smem + K3_WB + (it % 3) * 16384 + j * 8192 + wdst) = wreg[d][j];
    if (it + K3_D < NT) {
#pragma unroll
      for (int j = 0; j < 2; ++j) wreg[d][j] = *(const u32x4*)(wsrc + (size_t)(32 * j) * S_ + (it + K3_D) * 128);
    }
  };
  auto ldfrag = [&](bf16x8 (&bf)[4], int it, int k2) {
#pragma unroll
    for (int n = 0; n < 4; ++n)
      bf[n] = *(const bf16x8*)(smem + K3_WB + (it % 3) * 16384 + (16 * n + r) * 256 + (((4 * (2 * k2 + kh) + q) ^ r) * 16));
  };
  __builtin_amdgcn_sched_barrier(0);
  ldtile(2);
  __builtin_amdgcn_sched_barrier(0);
  stageW(0);
  __builtin_amdgcn_sched_barrier(0);
  ldtile(3);
  __builtin_amdgcn_sched_barrier(0);
  stageW(1);
  STAMP(2, 1);
  __syncthreads();
  STAMP(2, 2);

  f32x4 acc[2][4];
#pragma unroll
  for (int i = 0; i < 2; ++i)
#pragma unroll
    for (int n = 0; n < 4; ++n) acc[i][n] = (f32x4){0.f, 0.f, 0.f, 0.f};
  u32x2 zz1[2][2];
  float bsp[2];
  bf16x8 bfa[4], bfb[4];
  const int th = fb >> 2, dq = fb & 3;
  const int wv = wave < 7 ? wave : 6;
  bf16x8 wf[28];
  ldfrag(bfa, 0, 0);
#pragma unroll
  for (int it = 0; it < NT; ++it) {
    const int d = it % K3_D;
    ldfrag(bfb, it, 1);
    if (it + 2 < NT) stageW(it + 2);
    __builtin_amdgcn_sched_barrier(0);
#pragma unroll
    for (int n = 0; n < 4; ++n) {
      acc[0][n] = mfma16(zreg[d][0][0], bfa[n], acc[0][n]);
      acc[1][n] = mfma16(zreg[d][1][0], bfa[n], acc[1][n]);
    }
    __builtin_amdgcn_sched_barrier(0);
    if (it + 1 < NT) ldfrag(bfa, it + 1, 0);
    __builtin_amdgcn_sched_barrier(0);
#pragma unroll
    for (int n = 0; n < 4; ++n) {
      acc[0][n] = mfma16(zreg[d][0][1], bfb[n], acc[0][n]);
      acc[1][n] = mfma16(zreg[d][1][1], bfb[n], acc[1][n]);
    }
    __builtin_amdgcn_sched_barrier(0);
    if (it + K3_D < NT) {
#pragma unroll
      for (int k2 = 0; k2 < 2; ++k2) {
        zreg[d][0][k2] = *(const bf16x8*)(zsrc0 + ((it + K3_D) * 4 + 2 * k2) * 512);
        zreg[d][1][k2] = *(const bf16x8*)(zsrc1 + ((it + K3_D) * 4 + 2 * k2) * 512);
      }
    }
    if (it >= NT - 4) {
#pragma unroll
      for (int k7 = 0; k7 < 7; ++k7) {
        const int ks = 7 * (it - (NT - 4)) + k7;
        wf[ks] = *(const bf16x8*)(w_outT + (size_t)(((dq * 7 + wv) * 28 + ks) * 64 + lane) * 8);
      }
    }
    if (it == NT - K3_D) {
#pragma unroll
      for (int n = 0; n < 2; ++n) {
        bsp[n] = bf2f(b_sp[T0 + 16 * (2 * kh + n) + r]);
        zz1[0][n] = *(const u32x2*)(z1 + (size_t)(((tb * 4 + 2 * kh + n) * 56 + ft0) * 64 + lane) * 4);
        zz1[1][n] = *(const u32x2*)(z1 + (size_t)(((tb * 4 + 2 * kh + n) * 56 + ft1) * 64 + lane) * 4);
      }
    }
    __syncthreads();
  }
  STAMP(2, 3);
  {
    f32x4* xch = (f32x4*)smem;
#pragma unroll
    for (int i = 0; i < 2; ++i)
#pragma unroll
      for (int n = 0; n < 2; ++n) xch[((wave * 2 + i) * 2 + n) * 64 + lane] = kh ? acc[i][n] : acc[i][2 + n];
    __syncthreads();
#pragma unroll
    for (int i = 0; i < 2; ++i)
#pragma unroll
      for (int n = 0; n < 2; ++n) {
        const f32x4 o4 = xch[(((wave ^ 4) * 2 + i) * 2 + n) * 64 + lane];
        acc[i][n] = (kh ? acc[i][2 + n] : acc[i][n]) + o4;
      }
  }
  STAMP(2, 4);
  const __amdgpu_buffer_rsrc_t urs = __builtin_amdgcn_make_buffer_rsrc((void*)uout, 0, S_ * FH_ * 2, 0x00020000);
#pragma unroll
  for (int n = 0; n < 2; ++n) {
    const float bb = bsp[n];
#pragma unroll
    for (int i = 0; i < 2; ++i) {
      if (i == 1 && mp == 3) continue;
      const int ftile = i ? ft1 : ft0;
      const u32x2 zz = zz1[i][n];
      float g[4];
#pragma unroll
      for (int j = 0; j < 4; ++j) g[j] = rbf(rbf(acc[i][n][j]) + bb);
      u32x2 o = {pack2(lo2f(zz[0]) * g[0], hi2f(zz[0]) * g[1]), pack2(lo2f(zz[1]) * g[2], hi2f(zz[1]) * g[3])};
      const int off = ((((tb * 4 + 2 * kh + n) * 28 + (ftile >> 1)) * 64 + 16 * (2 * (ftile & 1) + (q >> 1)) + r) * 8 + 4 * (q & 1)) * 2;
      __builtin_amdgcn_raw_buffer_store_b64(o, urs, off, 0, 16);
    }
  }
  asm volatile("s_waitcnt vmcnt(0)" ::: "memory");
  __syncthreads();
  if (tid == 0) __hip_atomic_fetch_add(cnt + tb * 32, 1u, __ATOMIC_RELAXED, __HIP_MEMORY_SCOPE_AGENT);
  STAMP(2, 5);

  u32x2 hh[2];
#pragma unroll
  for (int n = 0; n < 2; ++n)
    hh[n] = *(const u32x2*)(hin + (size_t)(T0 + 32 * th + 16 * n + r) * D_ + dq * 112 + 16 * wv + 4 * q);
  if (tid == 0) {
    for (int spin = 0; spin < (1 << 22); ++spin) {
      const unsigned c = __hip_atomic_load(cnt + tb * 32, __ATOMIC_RELAXED, __HIP_MEMORY_SCOPE_AGENT);
      if (c >= 8u) break;
      __builtin_amdgcn_s_sleep(1);
    }
  }
  __syncthreads();
  STAMP(2, 6);
  {
    u32x4 ul[7];
#pragma unroll
    for (int j = 0; j < 7; ++j)
      ul[j] = __builtin_amdgcn_raw_buffer_load_b128(urs, (tb * 64 + th * 32) * FH_ * 2 + (tid + 512 * j) * 16, 0, 16);
#pragma unroll
    for (int j = 0; j < 7; ++j) *(u32x4*)(smem + (tid + 512 * j) * 16) = ul[j];
  }
  __syncthreads();
  STAMP(2, 7);
  if (wave < 7) {
    f32x4 ac2[2];
#pragma unroll
    for (int n = 0; n < 2; ++n) ac2[n] = (f32x4){0.f, 0.f, 0.f, 0.f};
#pragma unroll
    for (int ks = 0; ks < 28; ++ks) {
#pragma unroll
      for (int n = 0; n < 2; ++n) {
        const bf16x8 b = *(const bf16x8*)(smem + ((n * 28 + ks) * 64 + lane) * 16);
        ac2[n] = mfma16(wf[ks], b, ac2[n]);
      }
    }
#pragma unroll
    for (int n = 0; n < 2; ++n) {
      u32x2 o = {pack2(lo2f(hh[n][0]) + rbf(ac2[n][0]), hi2f(hh[n][0]) + rbf(ac2[n][1])),
                 pack2(lo2f(hh[n][1]) + rbf(ac2[n][2]), hi2f(hh[n][1]) + rbf(ac2[n][3]))};
      *(u32x2*)(out + (size_t)(T0 + 32 * th + 16 * n + r) * D_ + dq * 112 + 16 * wave + 4 * q) = o;
    }
  }
  STAMP(2, 8);
}

extern "C" void kernel_launch(void* const* d_in, const int* in_sizes, int n_in, void* d_out,
                              int out_size, void* d_ws, size_t ws_size, hipStream_t stream) {
  const u16* xq = (const u16*)d_in[0];
  const u16* kin = (const u16*)d_in[1];
  const u16* vin = (const u16*)d_in[2];
  const u16* shk = (const u16*)d_in[3];
  const u16* ssmk = (const u16*)d_in[4];
  const u16* ssmD = (const u16*)d_in[5];
  const u16* w_in = (const u16*)d_in[7];
  const u16* w_sp = (const u16*)d_in[8];
  const u16* b_sp = (const u16*)d_in[9];
  const u16* w_out = (const u16*)d_in[10];
  char* ws = (char*)d_ws;
  u16* yT = (u16*)(ws + WS_YT);
  u16* h = (u16*)(ws + WS_H);
  u16* w_inT = (u16*)(ws + WS_WINT);
  u16* w_outT = (u16*)(ws + WS_WOUTT);
  float2* ln1 = (float2*)(ws + WS_LN1);
  u16* z1 = (u16*)(ws + WS_Z1);
  u16* z2T = (u16*)(ws + WS_Z2T);
  unsigned long long* pst = (unsigned long long*)(ws + WS_PST);
  u16* kvr = (u16*)(ws + WS_KVR);
  u16* u = (u16*)(ws + WS_U);

  k0_prep<<<256, 512, 0, stream>>>(kin, vin, shk, kvr, xq, w_in, w_out, w_inT, w_outT, ln1, pst, ssmk, ws, w_sp);
  k1_conv<<<224, 512, 0, stream>>>(kvr, ssmk, ssmD, yT, w_sp);
  k2_gemm1<<<256, 512, 0, stream>>>(xq, yT, ln1, w_inT, h, z1, z2T, pst);
  k3_gemm2<<<256, 512, 0, stream>>>(w_sp, b_sp, z2T, z1, u, w_outT, h, (u16*)d_out, (unsigned*)(pst + 32 * 512));
}
```

```cpp
#include <hip/hip_runtime.h>
#include <hip/hip_bf16.h>
#include <stdint.h>

typedef __attribute__((ext_vector_type(8))) short bf16x8;
typedef __attribute__((ext_vector_type(4))) short s16x4;
typedef __attribute__((ext_vector_type(4))) float f32x4;
typedef __attribute__((ext_vector_type(2))) float f32x2;
typedef __attribute__((ext_vector_type(4))) unsigned int u32x4;
typedef __attribute__((ext_vector_type(2))) unsigned int u32x2;
typedef __attribute__((ext_vector_type(2))) __bf16 bf16x2_t;
typedef unsigned short u16;

#define DEVI __device__ __forceinline__
#define STAMP(kid, i)

constexpr int S_ = 2048, D_ = 448, F_ = 1792, FH_ = 896;

DEVI float lo2f(unsigned v) { return __uint_as_float(v << 16); }
DEVI float hi2f(unsigned v) { return __uint_as_float(v & 0xffff0000u); }
DEVI float bf2f(u16 v) { return __uint_as_float(((unsigned)v) << 16); }
DEVI unsigned pack2(float a, float b) {
  f32x2 f = {a, b};
  bf16x2_t h = __builtin_convertvector(f, bf16x2_t);
  return __builtin_bit_cast(unsigned, h);
}
DEVI float rbf(float x) { return lo2f(pack2(x, x)); }
DEVI f32x4 mfma16(bf16x8 a, bf16x8 b, f32x4 c) {
  return __builtin_amdgcn_mfma_f32_16x16x32_bf16(a, b, c, 0, 0, 0);
}
template <int CTRL> DEVI float dpp_f(float x) {
  return __builtin_bit_cast(float, __builtin_amdgcn_update_dpp(0, __builtin_bit_cast(int, x), CTRL, 0xf, 0xf, false));
}
DEVI float row16_sum(float x) {
  x += dpp_f<0x121>(x);
  x += dpp_f<0x122>(x);
  x += dpp_f<0x124>(x);
  x += dpp_f<0x128>(x);
  return x;
}
DEVI float gelu_tanh(float x) {
  const float u = x * (0.7978845608028654f + 0.035677408136300125f * x * x);
  const float e = __builtin_amdgcn_exp2f(-2.885390081777927f * u);
  return x * __builtin_amdgcn_rcpf(1.0f + e);
}

constexpr size_t WS_YT = 0;
constexpr size_t WS_H = WS_YT + (size_t)D_ * S_ * 2;
constexpr size_t WS_WINT = WS_H + (size_t)S_ * D_ * 2;
constexpr size_t WS_WOUTT = WS_WINT + (size_t)F_ * D_ * 2;
constexpr size_t WS_LN1 = WS_WOUTT + (size_t)D_ * FH_ * 2;
constexpr size_t WS_Z1 = WS_LN1 + (size_t)S_ * 8;
constexpr size_t WS_Z2T = WS_Z1 + (size_t)S_ * FH_ * 2;
constexpr size_t WS_PST = WS_Z2T + (size_t)S_ * FH_ * 2;
constexpr size_t WS_U = WS_PST + (size_t)9 * S_ * 8;
constexpr size_t WS_KVR = WS_U + (size_t)S_ * FH_ * 2;

constexpr int K1_XREV = 0;
constexpr int K1_XSTRIDE = 2304 * 2;
constexpr int K1_COP = 9216;
constexpr int K1_CSTRIDE = 4128;
constexpr int K1_RED = K1_COP + 2 * 8 * K1_CSTRIDE;
constexpr int K1_LDS = K1_RED + 8 * 8192;

DEVI float wave_sum(float x) {
  x = row16_sum(x);
  const int xi = __builtin_bit_cast(int, x);
  return __builtin_bit_cast(float, __builtin_amdgcn_readlane(xi, 0)) + __builtin_bit_cast(float, __builtin_amdgcn_readlane(xi, 16)) +
         __builtin_bit_cast(float, __builtin_amdgcn_readlane(xi, 32)) + __builtin_bit_cast(float, __builtin_amdgcn_readlane(xi, 48));
}

__global__ __launch_bounds__(512) void k0_prep(
    const u16* __restrict__ kin, const u16* __restrict__ vin, const u16* __restrict__ shk,
    u16* __restrict__ kvr,
    const u16* __restrict__ xin, const u16* __restrict__ w_in, const u16* __restrict__ w_out,
    u16* __restrict__ w_inT, u16* __restrict__ w_outT, float2* __restrict__ ln1,
    unsigned long long* __restrict__ gran, const u16* __restrict__ ssmk) {
  __shared__ __attribute__((aligned(16))) char smem[8 * 4608 + 64 * 144];
  const int tid = threadIdx.x, lane = tid & 63;
  const int wave = __builtin_amdgcn_readfirstlane(tid >> 6);
  const int blk = blockIdx.x;
  STAMP(4, 0);
  const int gw = blk * 8 + wave;
  const int r = lane & 15, q = lane >> 4;

  const bool has_tile = blk < 224;
  const int S0 = (blk / 7) * 64, D0 = (blk % 7) * 64;
  const int row = tid >> 3, c8 = (tid & 7) * 8;
  u32x4 kq[4], vq = {0u, 0u, 0u, 0u}, sk[4];
  if (has_tile) {
    const int s = S0 + row;
#pragma unroll
    for (int j = 0; j < 4; ++j) {
      kq[j] = (u32x4){0u, 0u, 0u, 0u};
      if (s - 3 + j >= 0) kq[j] = *(const u32x4*)(kin + (size_t)(s - 3 + j) * D_ + D0 + c8);
    }
    vq = *(const u32x4*)(vin + (size_t)s * D_ + D0 + c8);
#pragma unroll
    for (int j = 0; j < 4; ++j) sk[j] = *(const u32x4*)(shk + (D0 + c8) * 4 + j * 8);
  }
  u32x4 xr = {0u, 0u, 0u, 0u};
  if (lane < 56) xr = *(const u32x4*)(xin + (size_t)gw * D_ + lane * 8);
  const bool has_slab = gw < 588;
  u32x4 sl[4];
  if (has_slab) {
    const u16* src; int ld;
    if (gw < 392) { src = w_in + (size_t)(gw / 28) * 32 * F_ + (gw % 28) * 64; ld = F_; }
    else { const int i2 = gw - 392; src = w_out + (size_t)(i2 / 7) * 32 * D_ + (i2 % 7) * 64; ld = D_; }
#pragma unroll
    for (int i = 0; i < 4; ++i)
      sl[i] = *(const u32x4*)(src + (size_t)(8 * i + (lane >> 3)) * ld + (lane & 7) * 8);
  }
  const u32x4 pf = ((const u32x4*)ssmk)[blk < 224 ? blk * 512 + tid : tid];
  if (blk < 34) gran[blk * 512 + tid] = 0ull;

  STAMP(4, 1);
  u16* tl = (u16*)(smem + 8 * 4608);
  if (has_tile) {
#pragma unroll
    for (int e = 0; e < 8; ++e) {
      const unsigned s01 = sk[e >> 1][(e & 1) * 2], s23 = sk[e >> 1][(e & 1) * 2 + 1];
      float kf[4];
#pragma unroll
      for (int j = 0; j < 4; ++j) kf[j] = (e & 1) ? hi2f(kq[j][e >> 1]) : lo2f(kq[j][e >> 1]);
      float a = rbf(kf[0] * hi2f(s23));
      a = rbf(a + rbf(kf[1] * lo2f(s23)));
      a = rbf(a + rbf(kf[2] * hi2f(s01)));
      a = rbf(a + rbf(kf[3] * lo2f(s01)));
      const float vf = (e & 1) ? hi2f(vq[e >> 1]) : lo2f(vq[e >> 1]);
      tl[(c8 + e) * 72 + (63 - row)] = (u16)(pack2(a * vf, 0.f) & 0xffffu);
    }
  }
  STAMP(4, 2);
  {
    float v[8];
#pragma unroll
    for (int j = 0; j < 4; ++j) { v[2 * j] = lo2f(xr[j]); v[2 * j + 1] = hi2f(xr[j]); }
    float sm = 0.f;
#pragma unroll
    for (int j = 0; j < 8; ++j) sm += v[j];
    const float mean = wave_sum(sm) * (1.0f / D_);
    float m2 = 0.f;
    if (lane < 56) {
#pragma unroll
      for (int j = 0; j < 8; ++j) { float d = v[j] - mean; m2 += d * d; }
    }
    m2 = wave_sum(m2);
    if (lane == 0) {
      const float var = rbf(m2 * (1.0f / D_));
      const float sd = rbf(sqrtf(rbf(var + 1e-5f)));
      ln1[gw] = make_float2(rbf(mean), 1.0f / sd);
    }
  }
  STAMP(4, 3);
  if (has_slab) {
    typedef __attribute__((address_space(3))) s16x4* lp;
    char* ts = smem + wave * 4608;
    u16* dst; int bstride;
    if (gw < 392) { dst = w_inT + (size_t)((gw % 28) * 4 * 14 + (gw / 28)) * 512; bstride = 14 * 512; }
    else { const int i2 = gw - 392; dst = w_outT + (size_t)((i2 % 7) * 4 * 28 + (i2 / 7)) * 512; bstride = 28 * 512; }
#pragma unroll
    for (int i = 0; i < 4; ++i) *(u32x4*)(ts + (8 * i + (lane >> 3)) * 144 + (lane & 7) * 16) = sl[i];
#pragma unroll
    for (int n = 0; n < 4; ++n) {
      const char* a0p = ts + (8 * q + (r >> 2)) * 144 + (16 * n + 4 * (r & 3)) * 2;
      s16x4 v0 = __builtin_amdgcn_ds_read_tr16_b64_v4i16((lp)(a0p));
      s16x4 v1 = __builtin_amdgcn_ds_read_tr16_b64_v4i16((lp)(a0p + 4 * 144));
      bf16x8 o = {v0[0], v0[1], v0[2], v0[3], v1[0], v1[1], v1[2], v1[3]};
      *(bf16x8*)(dst + (size_t)n * bstride + lane * 8) = o;
    }
  }
  STAMP(4, 4);
  if (has_tile) {
    __syncthreads();
    const u32x4 o = *(const u32x4*)(tl + row * 72 + c8);
    *(u32x4*)(kvr + (size_t)(D0 + row) * S_ + (1984 - S0) + c8) = o;
  }
  asm volatile("" ::"v"(pf));
  STAMP(4, 5);
}

__global__ __launch_bounds__(512) void k1_conv(
    const u16* __restrict__ kvr, const u16* __restrict__ ssmk, const u16* __restrict__ ssmD,
    u16* __restrict__ yT, const u16* __restrict__ w_sp) {
  __shared__ __attribute__((aligned(16))) char smem[K1_LDS];
  const int tid = threadIdx.x, lane = tid & 63, wave = tid >> 6;
  STAMP(0, 0);
  const int xcd = blockIdx.x & 7, idx = blockIdx.x >> 3;
  const int d0 = (xcd * 28 + idx) * 2;
  const int tch = tid >> 8, mm = tid & 255;
  const u16* krow = ssmk + (size_t)(d0 + tch) * S_;
  const u32x4 xr = *(const u32x4*)(kvr + (size_t)(d0 + tch) * S_ + mm * 8);
  u32x4 wa = *(const u32x4*)(krow + mm * 8);
  u32x4 wb = {0u, 0u, 0u, 0u};
  if (mm < 255) wb = *(const u32x4*)(krow + mm * 8 + 8);
  const unsigned dDw = *(const unsigned*)(ssmD + d0);
  *(u32x4*)(smem + K1_XREV + tch * K1_XSTRIDE + mm * 16) = xr;

  if (tid < 64) {
    u32x4 z = {0u, 0u, 0u, 0u};
    *(u32x4*)(smem + K1_XREV + (tid >> 5) * K1_XSTRIDE + (2048 + (tid & 31) * 8) * 2) = z;
  }
  {
    unsigned w[8] = {wa[0], wa[1], wa[2], wa[3], wb[0], wb[1], wb[2], wb[3]};
    unsigned al[7];
#pragma unroll
    for (int i = 0; i < 7; ++i) al[i] = (w[i] >> 16) | (w[i + 1] << 16);
    char* base = smem + K1_COP + tch * 8 * K1_CSTRIDE + (mm + 2) * 16;
#pragma unroll
    for (int c = 0; c < 8; ++c) {
      u32x4 o;
      if ((c & 1) == 0) { o[0] = w[c / 2]; o[1] = w[c / 2 + 1]; o[2] = w[c / 2 + 2]; o[3] = w[c / 2 + 3]; }
      else { o[0] = al[c / 2]; o[1] = al[c / 2 + 1]; o[2] = al[c / 2 + 2]; o[3] = al[c / 2 + 3]; }
      *(u32x4*)(base + c * K1_CSTRIDE) = o;
    }
    if (mm < 2) {
      unsigned w2[8] = {0u, 0u, 0u, 0u, 0u, 0u, 0u, 0u};
      if (mm == 0) { w2[4] = wa[0]; w2[5] = wa[1]; w2[6] = wa[2]; w2[7] = wa[3]; }
      unsigned a2[7];
#pragma unroll
      for (int i = 0; i < 7; ++i) a2[i] = (w2[i] >> 16) | (w2[i + 1] << 16);
      char* b2 = smem + K1_COP + tch * 8 * K1_CSTRIDE + (1 - mm) * 16;
#pragma unroll
      for (int c = 0; c < 8; ++c) {
        u32x4 o;
        if ((c & 1) == 0) { o[0] = w2[c / 2]; o[1] = w2[c / 2 + 1]; o[2] = w2[c / 2 + 2]; o[3] = w2[c / 2 + 3]; }
        else { o[0] = a2[c / 2]; o[1] = a2[c / 2 + 1]; o[2] = a2[c / 2 + 2]; o[3] = a2[c / 2 + 3]; }
        *(u32x4*)(b2 + c * K1_CSTRIDE) = o;
      }
    }
  }
  STAMP(0, 1);
  __syncthreads();
  STAMP(0, 2);

  u32x4 pf[5];
  {
    const int base = (xcd * 28 + idx) * 2560 + tid;
#pragma unroll
    for (int j = 0; j < 5; ++j) {
      const int c = base + j * 512;
      pf[j] = ((const u32x4*)w_sp)[c < S_ * S_ / 8 ? c : 0];
    }
  }
  {
    const int ch = wave & 1, r = lane & 15, q = lane >> 4;
    f32x4 acc[8];
#pragma unroll
    for (int t = 0; t < 8; ++t) acc[t] = (f32x4){0.f, 0.f, 0.f, 0.f};
    const char* abase = smem + K1_COP + ch * 8 * K1_CSTRIDE + ((r + 1) & 7) * K1_CSTRIDE +
                        (2 * (q >> 1) - (q & 1) + ((r + 9) >> 3)) * 16;
    const char* bbase = smem + K1_XREV + ch * K1_XSTRIDE + (2040 - 16 * r + 16 * (q >> 1) - 8 * (q & 1)) * 2;
#pragma unroll
    for (int cc = 0; cc < 2; ++cc) {
      const int rho = 2 * (wave >> 1) + cc;
      bf16x8 A[8], B[8];
#pragma unroll
      for (int i = 0; i < 8; ++i) A[i] = *(const bf16x8*)(abase + (rho + 8 * i) * 64);
#pragma unroll
      for (int i = 0; i < 8; ++i) B[i] = *(const bf16x8*)(bbase - (8 * i - rho) * 64);
#pragma unroll
      for (int t = 0; t < 8; ++t)
#pragma unroll
        for (int i = 0; i <= t; ++i) acc[t] = mfma16(A[i], B[t - i], acc[t]);
    }
    float* red = (float*)(smem + K1_RED + wave * 8192);
#pragma unroll
    for (int t = 0; t < 8; ++t) *(f32x4*)(red + 256 * t + 16 * r + 4 * q) = acc[t];
  }
  STAMP(0, 3);
  __syncthreads();
  STAMP(0, 4);
  {
    const int ch = tid >> 8, e0 = (tid & 255) * 8;
    f32x4 sa = {0.f, 0.f, 0.f, 0.f}, sb = {0.f, 0.f, 0.f, 0.f};
#pragma unroll
    for (int w = 0; w < 4; ++w) {
      const float* red = (const float*)(smem + K1_RED + (2 * w + ch) * 8192);
      sa += *(const f32x4*)(red + e0);
      sb += *(const f32x4*)(red + e0 + 4);
    }
    const u32x4 kr = *(const u32x4*)(smem + K1_XREV + ch * K1_XSTRIDE + (2040 - e0) * 2);
    const float Dv = ch ? hi2f(dDw) : lo2f(dDw);
    float kvf[8];
    kvf[7] = lo2f(kr[0]); kvf[6] = hi2f(kr[0]); kvf[5] = lo2f(kr[1]); kvf[4] = hi2f(kr[1]);
    kvf[3] = lo2f(kr[2]); kvf[2] = hi2f(kr[2]); kvf[1] = lo2f(kr[3]); kvf[0] = hi2f(kr[3]);
    float y[8];
#pragma unroll
    for (int j = 0; j < 4; ++j) {
      y[j] = rbf(sa[j]) + rbf(kvf[j] * Dv);
      y[j + 4] = rbf(sb[j]) + rbf(kvf[j + 4] * Dv);
    }
    u32x4 o = {pack2(y[0], y[1]), pack2(y[2], y[3]), pack2(y[4], y[5]), pack2(y[6], y[7])};
    *(u32x4*)(yT + (size_t)(d0 + ch) * S_ + e0) = o;
  }
#pragma unroll
  for (int j = 0; j < 5; ++j) asm volatile("" ::"v"(pf[j]));
  STAMP(0, 5);
}

constexpr int K2_YT = 0;
constexpr int K2_AT = 448 * 80;
constexpr int K2_ST = K2_AT + 32 * 1024;
constexpr int K2_FS = K2_ST + 8 * 32 * 8;
constexpr int K2_LDS = K2_FS + 32 * 8;

DEVI f32x2 unp(unsigned v) { return (f32x2){lo2f(v), hi2f(v)}; }
DEVI unsigned pk(f32x2 v) { return pack2(v[0], v[1]); }

DEVI void phase2_gemm1(
    char* smem, const u16* __restrict__ xin, const u16* __restrict__ yT, const float2* __restrict__ ln1,
    const u16* __restrict__ w_inT, const __amdgpu_buffer_rsrc_t hrs, const __amdgpu_buffer_rsrc_t z1rs,
    const __amdgpu_buffer_rsrc_t z2rs, unsigned long long* pstat) {
  const int tid = threadIdx.x, lane = tid & 63;
  const int wave = __builtin_amdgcn_readfirstlane(tid >> 6);
  STAMP(1, 0);
  const int xcd = blockIdx.x & 7, idx = blockIdx.x >> 3;
  const int rb = xcd * 8 + (idx & 7), cb = idx >> 3;
  const int R0 = rb * 32;
  const int r = lane & 15, q = lane >> 4;

  u32x4 yl[4], xg[4];
#pragma unroll
  for (int j = 0; j < 4; ++j) {
    const int id = tid + 512 * j;
    if (j < 3 || tid < 256) {
      yl[j] = *(const u32x4*)(yT + (size_t)(id >> 2) * S_ + R0 + (id & 3) * 8);
      xg[j] = *(const u32x4*)(xin + (size_t)R0 * D_ + id * 8);
    }
  }
  const int prow = 16 * (wave & 1) + r;
  const int dq0 = (wave >> 1) * 28 + q;
  const float2 l1 = ln1[R0 + prow];
  const int wv = wave < 7 ? wave : 6;
  const u16* wz2 = w_inT + (size_t)((56 + cb * 14 + 2 * wv) * 14 * 64 + lane) * 8;
  const u16* wz1 = w_inT + (size_t)((cb * 14 + 2 * wv) * 14 * 64 + lane) * 8;
  bf16x8 wf[2][14];
#pragma unroll
  for (int ks = 0; ks < 7; ++ks)
#pragma unroll
    for (int i = 0; i < 2; ++i) wf[i][ks] = *(const bf16x8*)(wz2 + (i * 14 + ks) * 512);
#pragma unroll
  for (int j = 0; j < 4; ++j) {
    const int id = tid + 512 * j;
    if (j < 3 || tid < 256) {
      *(u32x4*)(smem + K2_YT + (id >> 2) * 80 + (id & 3) * 16) = yl[j];
      const int row = id / 56, c = id % 56;
      *(u32x4*)(smem + K2_AT + row * 1024 + ((c ^ (row & 15)) * 16)) = xg[j];
    }
  }
  STAMP(1, 1);
  __syncthreads();
  STAMP(1, 2);
  f32x2 hv[7][2];
  f32x2 sum2 = {0.f, 0.f};
  {
    typedef __attribute__((address_space(3))) s16x4* lp;
    const int qq = r >> 2, pp = r & 3;
    const f32x2 m1 = {l1.x, l1.x}, r1 = {l1.y, l1.y};
#pragma unroll
    for (int it = 0; it < 7; ++it) {
      const int dq = dq0 + 4 * it;
      const char* a0 = smem + K2_YT + (4 * dq + qq) * 80 + (16 * (wave & 1) + 4 * pp) * 2;
      const u32x2 y0 = __builtin_bit_cast(u32x2, __builtin_amdgcn_ds_read_tr16_b64_v4i16((lp)(a0)));
      const u32x2 xv = *(const u32x2*)(smem + K2_AT + prow * 1024 + (((dq >> 1) ^ r) * 16) + (dq & 1) * 8);
#pragma unroll
      for (int j = 0; j < 2; ++j) {
        const f32x2 x2 = unp(xv[j]);
        const unsigned dd = pk(x2 - m1);
        const unsigned qn = pk(unp(dd) * r1);
        const unsigned aa = pk(unp(qn) * unp(y0[j]));
        hv[it][j] = unp(pk(x2 + unp(aa)));
        sum2 += hv[it][j];
      }
#pragma unroll
      for (int i = 0; i < 2; ++i) wf[i][7 + it] = *(const bf16x8*)(wz2 + (i * 14 + 7 + it) * 512);
      __builtin_amdgcn_sched_barrier(0);
    }
  }
  float sum = sum2[0] + sum2[1];
  sum += __shfl_xor(sum, 16);
  sum += __shfl_xor(sum, 32);
  const float mh = sum * (1.0f / 112.0f);
  f32x2 q2 = {0.f, 0.f};
  {
    const f32x2 mh2 = {mh, mh};
#pragma unroll
    for (int it = 0; it < 7; ++it)
#pragma unroll
      for (int j = 0; j < 2; ++j) {
        const f32x2 d = hv[it][j] - mh2;
        q2 += d * d;
      }
  }
  float m2 = q2[0] + q2[1];
  m2 += __shfl_xor(m2, 16);
  m2 += __shfl_xor(m2, 32);
  float2* st = (float2*)(smem + K2_ST);
  if (q == 0) st[wave * 16 + r] = make_float2(mh, m2);
  STAMP(1, 3);
  __syncthreads();
  STAMP(1, 4);
  {
    float2 pq[4];
    float ms = 0.f;
#pragma unroll
    for (int w = 0; w < 4; ++w) { pq[w] = st[((wave & 1) + 2 * w) * 16 + r]; ms += pq[w].x; }
    const float mean = 0.25f * ms;
    float M2 = 0.f;
#pragma unroll
    for (int w = 0; w < 4; ++w) { const float dl = pq[w].x - mean; M2 += pq[w].y + 112.0f * dl * dl; }
    const float var = rbf(M2 * (1.0f / D_));
    const float mb = rbf(mean);
    const float rinv = 1.0f / rbf(sqrtf(rbf(var + 1e-5f)));
    const f32x2 mb2 = {mb, mb}, ri2 = {rinv, rinv};
#pragma unroll
    for (int it = 0; it < 7; ++it) {
      u32x2 o2;
#pragma unroll
      for (int j = 0; j < 2; ++j) o2[j] = pk(unp(pk(hv[it][j] - mb2)) * ri2);
      const int dq = dq0 + 4 * it;
      *(u32x2*)(smem + K2_AT + prow * 1024 + (((dq >> 1) ^ r) * 16) + (dq & 1) * 8) = o2;
      if (cb == 0) {
        u32x2 h2 = {pk(hv[it][0]), pk(hv[it][1])};
        __builtin_amdgcn_raw_buffer_store_b64(h2, hrs, ((R0 + prow) * D_ + dq * 4) * 2, 0, 16);
      }
    }
  }
  STAMP(1, 5);
  __syncthreads();
  STAMP(1, 6);

  u32x2 zp[2][2];
  bf16x8 wg[2][14];
  if (wave < 7) {
    f32x4 acc[2][2];
#pragma unroll
    for (int i = 0; i < 2; ++i)
#pragma unroll
      for (int s = 0; s < 2; ++s) acc[i][s] = (f32x4){0.f, 0.f, 0.f, 0.f};
#pragma unroll
    for (int ks = 0; ks < 14; ++ks) {
      bf16x8 xf[2];
#pragma unroll
      for (int s = 0; s < 2; ++s)
        xf[s] = *(const bf16x8*)(smem + K2_AT + (16 * s + r) * 1024 + (((4 * ks + q) ^ r) * 16));
#pragma unroll
      for (int i = 0; i < 2; ++i)
#pragma unroll
        for (int s = 0; s < 2; ++s) acc[i][s] = mfma16(xf[s], wf[i][ks], acc[i][s]);
#pragma unroll
      for (int i = 0; i < 2; ++i) wg[i][ks] = *(const bf16x8*)(wz1 + (i * 14 + ks) * 512);
    }
#pragma unroll
    for (int s = 0; s < 2; ++s) {
#pragma unroll
      for (int i = 0; i < 2; ++i) {
        float zz[4];
#pragma unroll
        for (int j = 0; j < 4; ++j) zz[j] = gelu_tanh(rbf(acc[i][s][j]));
        zp[i][s] = (u32x2){pack2(zz[0], zz[1]), pack2(zz[2], zz[3])};
      }
      float mw[4], qw[4];
#pragma unroll
      for (int j = 0; j < 4; ++j) {
        const float v0 = (j & 1) ? hi2f(zp[0][s][j >> 1]) : lo2f(zp[0][s][j >> 1]);
        const float v1 = (j & 1) ? hi2f(zp[1][s][j >> 1]) : lo2f(zp[1][s][j >> 1]);
        mw[j] = row16_sum(v0 + v1) * (1.0f / 32.0f);
        const float a0 = v0 - mw[j], a1 = v1 - mw[j];
        qw[j] = row16_sum(a0 * a0 + a1 * a1);
      }
      if (r == 0) {
#pragma unroll
        for (int j = 0; j < 4; ++j) st[wave * 32 + 16 * s + 4 * q + j] = make_float2(mw[j], qw[j]);
      }
    }
  }
  __syncthreads();
  if (wave == 7) {
    float2* fs = (float2*)(smem + K2_FS);
    const int row = lane >> 1;
    float msum = 0.f;
    float2 pw[7];
#pragma unroll
    for (int w = 0; w < 7; ++w) { pw[w] = st[w * 32 + row]; msum += pw[w].x; }
    const float bmean = msum * (1.0f / 7.0f);
    float bM2 = 0.f;
#pragma unroll
    for (int w = 0; w < 7; ++w) { const float dl = pw[w].x - bmean; bM2 += pw[w].y + 32.0f * dl * dl; }
    const float mine = (lane & 1) ? bM2 : bmean;
    unsigned long long* g = pstat + (size_t)rb * 256 + lane;
    __hip_atomic_store(g + cb * 64, (1ull << 32) | __float_as_uint(mine), __ATOMIC_RELAXED, __HIP_MEMORY_SCOPE_AGENT);
    float pv[4];
    for (int spin = 0; spin < (1 << 22); ++spin) {
      bool ok = true;
#pragma unroll
      for (int c = 0; c < 4; ++c) {
        const unsigned long long v = __hip_atomic_load(g + c * 64, __ATOMIC_RELAXED, __HIP_MEMORY_SCOPE_AGENT);
        pv[c] = __uint_as_float((unsigned)v);
        ok &= (v >> 32) == 1ull;
      }
      if (__all(ok)) break;
      __builtin_amdgcn_s_sleep(1);
    }
    float pm[4], pq[4];
#pragma unroll
    for (int c = 0; c < 4; ++c) {
      const float o = __shfl_xor(pv[c], 1);
      pm[c] = (lane & 1) ? o : pv[c];
      pq[c] = (lane & 1) ? pv[c] : o;
    }
    const float mean = (pm[0] + pm[1] + pm[2] + pm[3]) * 0.25f;
    float M2 = 0.f;
#pragma unroll
    for (int c = 0; c < 4; ++c) { const float dl = pm[c] - mean; M2 += pq[c] + 224.0f * dl * dl; }
    const float var = rbf(M2 * (1.0f / FH_));
    if ((lane & 1) == 0) fs[row] = make_float2(rbf(mean), 1.0f / rbf(sqrtf(rbf(var + 1e-5f))));
  } else {
    STAMP(1, 7);
    f32x4 acc[2][2];
#pragma unroll
    for (int i = 0; i < 2; ++i)
#pragma unroll
      for (int s = 0; s < 2; ++s) acc[i][s] = (f32x4){0.f, 0.f, 0.f, 0.f};
#pragma unroll
    for (int ks = 0; ks < 14; ++ks) {
      bf16x8 xf[2];
#pragma unroll
      for (int s = 0; s < 2; ++s)
        xf[s] = *(const bf16x8*)(smem + K2_AT + (16 * s + r) * 1024 + (((4 * ks + q) ^ r) * 16));
#pragma unroll
      for (int i = 0; i < 2; ++i)
#pragma unroll
        for (int s = 0; s < 2; ++s) acc[i][s] = mfma16(wg[i][ks], xf[s], acc[i][s]);
    }
#pragma unroll
    for (int s = 0; s < 2; ++s)
#pragma unroll
      for (int i = 0; i < 2; ++i) {
        float zz[4];
#pragma unroll
        for (int j = 0; j < 4; ++j) zz[j] = gelu_tanh(rbf(acc[i][s][j]));
        u32x2 o = {pack2(zz[0], zz[1]), pack2(zz[2], zz[3])};
        __builtin_amdgcn_raw_buffer_store_b64(o, z1rs, ((((rb * 2 + s) * 56 + cb * 14 + 2 * wave + i) * 64 + lane) * 4) * 2, 0, 16);
      }
    STAMP(1, 8);
  }
  __syncthreads();
  STAMP(1, 9);
  if (wave < 7) {
    const float2* fs = (const float2*)(smem + K2_FS);
#pragma unroll
    for (int s = 0; s < 2; ++s) {
      const f32x4 sa = *(const f32x4*)(fs + 16 * s + 4 * q), sb = *(const f32x4*)(fs + 16 * s + 4 * q + 2);
#pragma unroll
      for (int i = 0; i < 2; ++i) {
        const float n0 = rbf(lo2f(zp[i][s][0]) - sa[0]) * sa[1], n1 = rbf(hi2f(zp[i][s][0]) - sa[2]) * sa[3];
        const float n2 = rbf(lo2f(zp[i][s][1]) - sb[0]) * sb[1], n3 = rbf(hi2f(zp[i][s][1]) - sb[2]) * sb[3];
        u32x2 o = {pack2(n0, n1), pack2(n2, n3)};
        const int ftile = cb * 14 + 2 * wave + i;
        const int lq = 2 * s + (q >> 1);
        __builtin_amdgcn_raw_buffer_store_b64(o, z2rs, (((ftile * 64 + rb) * 64 + 16 * lq + r) * 8 + 4 * (q & 1)) * 2, 0, 16);
      }
    }
  }
  STAMP(1, 10);
}

constexpr int K3_WB = 0;
constexpr int K3_LDS = 32 * 1792;
constexpr int K3_D = 4;

DEVI void phase3_gemm2(
    char* smem, const u16* __restrict__ w_sp, const u16* __restrict__ b_sp, const __amdgpu_buffer_rsrc_t z2rs,
    const __amdgpu_buffer_rsrc_t z1rs, u16* uout, const u16* __restrict__ w_outT,
    const __amdgpu_buffer_rsrc_t hrs, u16* __restrict__ out, unsigned* cnt, unsigned* gcnt) {
  const int tid = threadIdx.x, lane = tid & 63;
  const int wave = __builtin_amdgcn_readfirstlane(tid >> 6);
  STAMP(2, 0);
  const int xcd = blockIdx.x & 7, idx = blockIdx.x >> 3;
  const int tb = (xcd & 3) * 8 + (idx & 7), fb = (xcd >> 2) * 4 + (idx >> 3);
  const int T0 = tb * 64;
  const int r = lane & 15, q = lane >> 4;
  const int kh = wave >> 2, mp = wave & 3;
  constexpr int NT = 16;

  const u16* wsrc = w_sp + (size_t)(T0 + (tid >> 4)) * S_ + (tid & 15) * 8;
  const int ft0 = fb * 7 + 2 * mp, ft1 = fb * 7 + (mp < 3 ? 2 * mp + 1 : 2 * mp);
  const int zoff0 = ((ft0 * 64 + kh) * 64 + lane) * 16, zoff1 = ((ft1 * 64 + kh) * 64 + lane) * 16;
  auto ldz = [&](int off) { return __builtin_bit_cast(bf16x8, __builtin_amdgcn_raw_buffer_load_b128(z2rs, off, 0, 16)); };
  const int wdst = (tid >> 4) * 256 + (((tid & 15) ^ ((tid >> 4) & 15)) * 16);

  u32x4 wreg[K3_D][2];
  bf16x8 zreg[K3_D][2][2];
#pragma unroll
  for (int d = 0; d < K3_D; ++d)
#pragma unroll
    for (int j = 0; j < 2; ++j) wreg[d][j] = *(const u32x4*)(wsrc + (size_t)(32 * j) * S_ + d * 128);
  auto stageW = [&](int it) {
    const int d = it % K3_D;
#pragma unroll
    for (int j = 0; j < 2; ++j) *(u32x4*)(smem + K3_WB + (it % 3) * 16384 + j * 8192 + wdst) = wreg[d][j];
    if (it + K3_D < NT) {
#pragma unroll
      for (int j = 0; j < 2; ++j) wreg[d][j] = *(const u32x4*)(wsrc + (size_t)(32 * j) * S_ + (it + K3_D) * 128);
    }
  };
  auto ldfrag = [&](bf16x8 (&bf)[4], int it, int k2) {
#pragma unroll
    for (int n = 0; n < 4; ++n)
      bf[n] = *(const bf16x8*)(smem + K3_WB + (it % 3) * 16384 + (16 * n + r) * 256 + (((4 * (2 * k2 + kh) + q) ^ r) * 16));
  };
  stageW(0);
  stageW(1);
  if (wave == 0) {
    for (int spin = 0; spin < (1 << 22); ++spin) {
      const unsigned c = __hip_atomic_load(gcnt + (lane & 7) * 32, __ATOMIC_RELAXED, __HIP_MEMORY_SCOPE_AGENT);
      if (__all(c >= 32u)) break;
      __builtin_amdgcn_s_sleep(1);
    }
  }
  __syncthreads();
#pragma unroll
  for (int d = 0; d < K3_D; ++d)
#pragma unroll
    for (int k2 = 0; k2 < 2; ++k2) {
      zreg[d][0][k2] = ldz(zoff0 + (d * 4 + 2 * k2) * 1024);
      zreg[d][1][k2] = ldz(zoff1 + (d * 4 + 2 * k2) * 1024);
    }
  STAMP(2, 1);
  STAMP(2, 2);

  f32x4 acc[2][4];
#pragma unroll
  for (int i = 0; i < 2; ++i)
#pragma unroll
    for (int n = 0; n < 4; ++n) acc[i][n] = (f32x4){0.f, 0.f, 0.f, 0.f};
  u32x2 zz1[2][2];
  float bsp[2];
  bf16x8 bfa[4], bfb[4];
  const int th = fb >> 2, dq = fb & 3;
  const int wv = wave < 7 ? wave : 6;
  bf16x8 wf[28];
  ldfrag(bfa, 0, 0);
#pragma unroll
  for (int it = 0; it < NT; ++it) {
    const int d = it % K3_D;
    ldfrag(bfb, it, 1);
    if (it + 2 < NT) stageW(it + 2);
    __builtin_amdgcn_sched_barrier(0);
#pragma unroll
    for (int n = 0; n < 4; ++n) {
      acc[0][n] = mfma16(zreg[d][0][0], bfa[n], acc[0][n]);
      acc[1][n] = mfma16(zreg[d][1][0], bfa[n], acc[1][n]);
    }
    __builtin_amdgcn_sched_barrier(0);
    if (it + 1 < NT) ldfrag(bfa, it + 1, 0);
    __builtin_amdgcn_sched_barrier(0);
#pragma unroll
    for (int n = 0; n < 4; ++n) {
      acc[0][n] = mfma16(zreg[d][0][1], bfb[n], acc[0][n]);
      acc[1][n] = mfma16(zreg[d][1][1], bfb[n], acc[1][n]);
    }
    __builtin_amdgcn_sched_barrier(0);
    if (it + K3_D < NT) {
#pragma unroll
      for (int k2 = 0; k2 < 2; ++k2) {
        zreg[d][0][k2] = ldz(zoff0 + ((it + K3_D) * 4 + 2 * k2) * 1024);
        zreg[d][1][k2] = ldz(zoff1 + ((it + K3_D) * 4 + 2 * k2) * 1024);
      }
    }
    if (it >= NT - 4) {
#pragma unroll
      for (int k7 = 0; k7 < 7; ++k7) {
        const int ks = 7 * (it - (NT - 4)) + k7;
        wf[ks] = *(const bf16x8*)(w_outT + (size_t)(((dq * 7 + wv) * 28 + ks) * 64 + lane) * 8);
      }
    }
    if (it == NT - K3_D) {
#pragma unroll
      for (int n = 0; n < 2; ++n) {
        bsp[n] = bf2f(b_sp[T0 + 16 * (2 * kh + n) + r]);
        zz1[0][n] = __builtin_amdgcn_raw_buffer_load_b64(z1rs, ((((tb * 4 + 2 * kh + n) * 56 + ft0) * 64 + lane) * 4) * 2, 0, 16);
        zz1[1][n] = __builtin_amdgcn_raw_buffer_load_b64(z1rs, ((((tb * 4 + 2 * kh + n) * 56 + ft1) * 64 + lane) * 4) * 2, 0, 16);
      }
    }
    __syncthreads();
  }
  STAMP(2, 3);
  {
    f32x4* xch = (f32x4*)smem;
#pragma unroll
    for (int i = 0; i < 2; ++i)
#pragma unroll
      for (int n = 0; n < 2; ++n) xch[((wave * 2 + i) * 2 + n) * 64 + lane] = kh ? acc[i][n] : acc[i][2 + n];
    __syncthreads();
#pragma unroll
    for (int i = 0; i < 2; ++i)
#pragma unroll
      for (int n = 0; n < 2; ++n) {
        const f32x4 o4 = xch[(((wave ^ 4) * 2 + i) * 2 + n) * 64 + lane];
        acc[i][n] = (kh ? acc[i][2 + n] : acc[i][n]) + o4;
      }
  }
  STAMP(2, 4);
  const __amdgpu_buffer_rsrc_t urs = __builtin_amdgcn_make_buffer_rsrc((void*)uout, 0, S_ * FH_ * 2, 0x00020000);
#pragma unroll
  for (int n = 0; n < 2; ++n) {
    const float bb = bsp[n];
#pragma unroll
    for (int i = 0; i < 2; ++i) {
      if (i == 1 && mp == 3) continue;
      const int ftile = i ? ft1 : ft0;
      const u32x2 zz = zz1[i][n];
      float g[4];
#pragma unroll
      for (int j = 0; j < 4; ++j) g[j] = rbf(rbf(acc[i][n][j]) + bb);
      u32x2 o = {pack2(lo2f(zz[0]) * g[0], hi2f(zz[0]) * g[1]), pack2(lo2f(zz[1]) * g[2], hi2f(zz[1]) * g[3])};
      const int off = ((((tb * 4 + 2 * kh + n) * 28 + (ftile >> 1)) * 64 + 16 * (2 * (ftile & 1) + (q >> 1)) + r) * 8 + 4 * (q & 1)) * 2;
      __builtin_amdgcn_raw_buffer_store_b64(o, urs, off, 0, 16);
    }
  }
  asm volatile("s_waitcnt vmcnt(0)" ::: "memory");
  __syncthreads();
  if (tid == 0) __hip_atomic_fetch_add(cnt + tb * 32, 1u, __ATOMIC_RELAXED, __HIP_MEMORY_SCOPE_AGENT);
  STAMP(2, 5);

  u32x2 hh[2];
#pragma unroll
  for (int n = 0; n < 2; ++n)
    hh[n] = __builtin_amdgcn_raw_buffer_load_b64(hrs, ((T0 + 32 * th + 16 * n + r) * D_ + dq * 112 + 16 * wv + 4 * q) * 2, 0, 16);
  if (tid == 0) {
    for (int spin = 0; spin < (1 << 22); ++spin) {
      const unsigned c = __hip_atomic_load(cnt + tb * 32, __ATOMIC_RELAXED, __HIP_MEMORY_SCOPE_AGENT);
      if (c >= 8u) break;
      __builtin_amdgcn_s_sleep(1);
    }
  }
  __syncthreads();
  STAMP(2, 6);
  {
    u32x4 ul[7];
#pragma unroll
    for (int j = 0; j < 7; ++j)
      ul[j] = __builtin_amdgcn_raw_buffer_load_b128(urs, (tb * 64 + th * 32) * FH_ * 2 + (tid + 512 * j) * 16, 0, 16);
#pragma unroll
    for (int j = 0; j < 7; ++j) *(u32x4*)(smem + (tid + 512 * j) * 16) = ul[j];
  }
  __syncthreads();
  STAMP(2, 7);
  if (wave < 7) {
    f32x4 ac2[2];
#pragma unroll
    for (int n = 0; n < 2; ++n) ac2[n] = (f32x4){0.f, 0.f, 0.f, 0.f};
#pragma unroll
    for (int ks = 0; ks < 28; ++ks) {
#pragma unroll
      for (int n = 0; n < 2; ++n) {
        const bf16x8 b = *(const bf16x8*)(smem + ((n * 28 + ks) * 64 + lane) * 16);
        ac2[n] = mfma16(wf[ks], b, ac2[n]);
      }
    }
#pragma unroll
    for (int n = 0; n < 2; ++n) {
      u32x2 o = {pack2(lo2f(hh[n][0]) + rbf(ac2[n][0]), hi2f(hh[n][0]) + rbf(ac2[n][1])),
                 pack2(lo2f(hh[n][1]) + rbf(ac2[n][2]), hi2f(hh[n][1]) + rbf(ac2[n][3]))};
      *(u32x2*)(out + (size_t)(T0 + 32 * th + 16 * n + r) * D_ + dq * 112 + 16 * wave + 4 * q) = o;
    }
  }
  STAMP(2, 8);
}

__global__ __launch_bounds__(512) void k23_mlp(
    const u16* __restrict__ xin, const u16* __restrict__ yT, const float2* __restrict__ ln1,
    const u16* __restrict__ w_inT, u16* h, u16* z1, u16* z2T, unsigned long long* pstat,
    const u16* __restrict__ w_sp, const u16* __restrict__ b_sp, u16* uout, const u16* __restrict__ w_outT,
    u16* __restrict__ out) {
  __shared__ __attribute__((aligned(16))) char smem[K2_LDS > K3_LDS ? K2_LDS : K3_LDS];
  const __amdgpu_buffer_rsrc_t hrs = __builtin_amdgcn_make_buffer_rsrc((void*)h, 0, S_ * D_ * 2, 0x00020000);
  const __amdgpu_buffer_rsrc_t z1rs = __builtin_amdgcn_make_buffer_rsrc((void*)z1, 0, S_ * FH_ * 2, 0x00020000);
  const __amdgpu_buffer_rsrc_t z2rs = __builtin_amdgcn_make_buffer_rsrc((void*)z2T, 0, S_ * FH_ * 2, 0x00020000);
  unsigned* cnt = (unsigned*)(pstat + 32 * 512);
  unsigned* gcnt = (unsigned*)(pstat + 33 * 512);
  phase2_gemm1(smem, xin, yT, ln1, w_inT, hrs, z1rs, z2rs, pstat);
  asm volatile("s_waitcnt vmcnt(0)" ::: "memory");
  __syncthreads();
  if (threadIdx.x == 0)
    __hip_atomic_fetch_add(gcnt + (blockIdx.x & 7) * 32, 1u, __ATOMIC_RELAXED, __HIP_MEMORY_SCOPE_AGENT);
  phase3_gemm2(smem, w_sp, b_sp, z2rs, z1rs, uout, w_outT, hrs, out, cnt, gcnt);
}

extern "C" void kernel_launch(void* const* d_in, const int* in_sizes, int n_in, void* d_out,
                              int out_size, void* d_ws, size_t ws_size, hipStream_t stream) {
  const u16* xq = (const u16*)d_in[0];
  const u16* kin = (const u16*)d_in[1];
  const u16* vin = (const u16*)d_in[2];
  const u16* shk = (const u16*)d_in[3];
  const u16* ssmk = (const u16*)d_in[4];
  const u16* ssmD = (const u16*)d_in[5];
  const u16* w_in = (const u16*)d_in[7];
  const u16* w_sp = (const u16*)d_in[8];
  const u16* b_sp = (const u16*)d_in[9];
  const u16* w_out = (const u16*)d_in[10];
  char* ws = (char*)d_ws;
  u16* yT = (u16*)(ws + WS_YT);
  u16* h = (u16*)(ws + WS_H);
  u16* w_inT = (u16*)(ws + WS_WINT);
  u16* w_outT = (u16*)(ws + WS_WOUTT);
  float2* ln1 = (float2*)(ws + WS_LN1);
  u16* z1 = (u16*)(ws + WS_Z1);
  u16* z2T = (u16*)(ws + WS_Z2T);
  unsigned long long* pst = (unsigned long long*)(ws + WS_PST);
  u16* kvr = (u16*)(ws + WS_KVR);
  u16* u = (u16*)(ws + WS_U);

  k0_prep<<<256, 512, 0, stream>>>(kin, vin, shk, kvr, xq, w_in, w_out, w_inT, w_outT, ln1, pst, ssmk);
  k1_conv<<<224, 512, 0, stream>>>(kvr, ssmk, ssmD, yT, w_sp);
  k23_mlp<<<256, 512, 0, stream>>>(xq, yT, ln1, w_inT, h, z1, z2T, pst, w_sp, b_sp, u, w_outT, (u16*)d_out);
}
```
